```python
import torch

def _softmax(x, axis=-1):
    return torch.softmax(x, dim=axis)
def _gelu(x):
    return torch.nn.functional.gelu(x, approximate="tanh")
def _silu(x):
    return torch.nn.functional.silu(x)
def _sigmoid(x):
    return torch.sigmoid(x)
def _layernorm(x, axis=-1, eps=1e-5):
    m = x.mean(dim=axis, keepdim=True)
    v = x.var(dim=axis, keepdim=True, unbiased=False)
    return (x - m) / torch.sqrt(v + eps)
def _scatter_add(tgt, idx, src, axis=0):
    return tgt.clone().index_add_(axis, idx, src)
def _segment_sum(src, seg, n):
    out = torch.zeros((n,) + src.shape[1:], dtype=src.dtype)
    return out.index_add_(0, seg, src)
def _pad(x, pw):
    flat = tuple(v for pair in reversed(pw) for v in pair)
    return torch.nn.functional.pad(x, flat)

kernel_name = 'gated_mix__sliding_block__dense_activation_relu__prompt'

def setup_inputs():

    _w = (lambda a: (lambda t: t.to(torch.bfloat16) if t.is_floating_point() else t)(torch.as_tensor(a)))
    def _A_setup():
        B, S, D = 1, 2048, 640
        return {
            "x": _w((lambda *s: torch.randn(*s, dtype=torch.bfloat16) * 0.05)(B, S, D)),
            "wqkv": _w((lambda *s: torch.randn(*s, dtype=torch.bfloat16) * 1.6)(D, 3 * D)),
            "wo": _w((lambda *s: torch.randn(*s, dtype=torch.bfloat16) * 0.05)(D, D)),
        }
    _A_in = _A_setup()

    def _B_setup():
        B, S, D, Dff = 1, 2048, 640, 1536
        return {
            "x": _w((lambda *s: torch.randn(*s, dtype=torch.bfloat16) * 0.05)(B, S, D)),
            "w1": _w((lambda *s: torch.randn(*s, dtype=torch.bfloat16) * 0.2)(D, Dff)),
            "w2": _w((lambda *s: torch.randn(*s, dtype=torch.bfloat16) * 0.2)(Dff, D)),
        }
    _B_in = _B_setup()

    return {
        "A_x": _A_in["x"],
        "A_wqkv": _A_in["wqkv"],
        "A_wo": _A_in["wo"],
        "B_x": _B_in["x"],
        "B_w1": _B_in["w1"],
        "B_w2": _B_in["w2"],
        "gate_w": _w((lambda *s: torch.randn(*s, dtype=torch.bfloat16) * 0.05)(640) * 16.0),
    }

def reference(A_x, A_wqkv, A_wo, B_x, B_w1, B_w2, gate_w):
    def _A_ref(x, wqkv, wo):
        B, S, D = x.shape
        H = 32; Dh = D // H; W = 256
        qkv = (lambda x, *d: x.permute(*d))((x @ wqkv).reshape(B, S, 3, H, Dh), 2, 0, 3, 1, 4)
        q, k, v = qkv[0], qkv[1], qkv[2]
        scores = torch.einsum("bhqd,bhkd->bhqk", q, k) * (Dh ** -0.5)
        idx = torch.arange(S)
        mask = (idx[:, None] - idx[None, :] >= 0) & (idx[:, None] - idx[None, :] < W)
        scores = torch.where(mask[None, None], scores, -1e9)
        p = _softmax(scores, axis=-1)
        o = torch.einsum("bhqk,bhkd->bhqd", p, v)
        return (lambda x, *d: x.permute(*d))(o, 0, 2, 1, 3).reshape(B, S, D) @ wo

    def _B_ref(x, w1, w2):
        h = torch.einsum("bsd,df->bsf", x, w1)
        zero = torch.zeros_like(h)
        h = torch.maximum(h, zero)
        return torch.einsum("bsf,fd->bsd", h, w2)

    x = A_x
    g = _sigmoid((x * gate_w).sum(-1, keepdims=True))
    a = _A_ref(x, A_wqkv, A_wo)
    b = _B_ref(x, B_w1, B_w2)
    return g * a + (1 - g) * b
```

```cpp
#include <hip/hip_runtime.h>
#include <hip/hip_bf16.h>
#include <stdint.h>

typedef unsigned short u16;
__device__ __forceinline__ float bf2f(u16 v) { return __uint_as_float(((unsigned)v) << 16); }
__device__ __forceinline__ u16 f2bf(float f) { __hip_bfloat16 b = __float2bfloat16(f); return *reinterpret_cast<u16*>(&b); }

constexpr int S = 2048, D = 640, H = 32, DH = 20, W = 256, DFF = 1536;

__global__ void gemm_naive(const u16* A, const u16* B, u16* C, int M, int N, int K, int relu) {
    int n = blockIdx.x * blockDim.x + threadIdx.x;
    int m = blockIdx.y;
    if (n >= N) return;
    float acc = 0.f;
    for (int k = 0; k < K; ++k) acc += bf2f(A[m * K + k]) * bf2f(B[k * N + n]);
    if (relu) acc = fmaxf(acc, 0.f);
    C[m * N + n] = f2bf(acc);
}

__global__ void attn_naive(const u16* qkv, u16* o) {
    int q = blockIdx.x;
    int h = threadIdx.x;
    if (h >= H) return;
    float qv[DH];
#pragma unroll
    for (int d = 0; d < DH; ++d) qv[d] = bf2f(qkv[q * 1920 + h * DH + d]);
    int k0 = q - (W - 1); if (k0 < 0) k0 = 0;
    float m = -1e30f;
    const float sc = 0.22360679774997896f;
    for (int k = k0; k <= q; ++k) {
        float s = 0.f;
#pragma unroll
        for (int d = 0; d < DH; ++d) s += qv[d] * bf2f(qkv[k * 1920 + 640 + h * DH + d]);
        m = fmaxf(m, s * sc);
    }
    float l = 0.f; float acc[DH];
#pragma unroll
    for (int d = 0; d < DH; ++d) acc[d] = 0.f;
    for (int k = k0; k <= q; ++k) {
        float s = 0.f;
#pragma unroll
        for (int d = 0; d < DH; ++d) s += qv[d] * bf2f(qkv[k * 1920 + 640 + h * DH + d]);
        float p = __expf(s * sc - m);
        l += p;
#pragma unroll
        for (int d = 0; d < DH; ++d) acc[d] += p * bf2f(qkv[k * 1920 + 1280 + h * DH + d]);
    }
#pragma unroll
    for (int d = 0; d < DH; ++d) o[q * D + h * DH + d] = f2bf(acc[d] / l);
}

__global__ void combine(const u16* x, const u16* gw, const u16* a, const u16* b, u16* out) {
    int m = blockIdx.x;
    __shared__ float red[64];
    float s = 0.f;
    for (int k = threadIdx.x; k < D; k += 64) s += bf2f(f2bf(bf2f(x[m * D + k]) * bf2f(gw[k])));
    for (int off = 32; off; off >>= 1) s += __shfl_xor(s, off);
    s = bf2f(f2bf(s));
    float g = bf2f(f2bf(1.f / (1.f + __expf(-s))));
    float og = bf2f(f2bf(1.f - g));
    for (int n = threadIdx.x; n < D; n += 64) {
        float t1 = bf2f(f2bf(g * bf2f(a[m * D + n])));
        float t2 = bf2f(f2bf(og * bf2f(b[m * D + n])));
        out[m * D + n] = f2bf(t1 + t2);
    }
}

extern "C" void kernel_launch(void* const* d_in, const int* in_sizes, int n_in,
                              void* d_out, int out_size, void* d_ws, size_t ws_size,
                              hipStream_t stream) {
    const u16* x = (const u16*)d_in[0];
    const u16* wqkv = (const u16*)d_in[1];
    const u16* wo = (const u16*)d_in[2];
    const u16* w1 = (const u16*)d_in[4];
    const u16* w2 = (const u16*)d_in[5];
    const u16* gw = (const u16*)d_in[6];
    u16* ws = (u16*)d_ws;
    u16* qkv = ws; u16* h = qkv + S * 1920; u16* o = h + S * DFF; u16* a = o + S * D; u16* b = a + S * D;
    gemm_naive<<<dim3(1920 / 64, S), 64, 0, stream>>>(x, wqkv, qkv, S, 1920, D, 0);
    gemm_naive<<<dim3(DFF / 64, S), 64, 0, stream>>>(x, w1, h, S, DFF, D, 1);
    attn_naive<<<S, 64, 0, stream>>>(qkv, o);
    gemm_naive<<<dim3(D / 64, S), 64, 0, stream>>>(o, wo, a, S, D, D, 0);
    gemm_naive<<<dim3(D / 64, S), 64, 0, stream>>>(h, w2, b, S, D, DFF, 0);
    combine<<<S, 64, 0, stream>>>(x, gw, a, b, (u16*)d_out);
}
```

```cpp
#include <hip/hip_runtime.h>
#include <stdint.h>

typedef unsigned short u16;
typedef __attribute__((ext_vector_type(8))) __bf16 bf16x8;
typedef __attribute__((ext_vector_type(2))) __bf16 bf16x2;
typedef __attribute__((ext_vector_type(4))) short s16x4;
typedef __attribute__((ext_vector_type(4))) float f32x4;
typedef __attribute__((ext_vector_type(2))) float f32x2;
typedef __attribute__((ext_vector_type(4))) unsigned u32x4;
typedef __attribute__((ext_vector_type(2))) unsigned u32x2;
#define LAS __attribute__((address_space(3)))
#define GAS __attribute__((address_space(1)))
#define DI __device__ __forceinline__
typedef LAS char* lptr;

constexpr int SEQ = 2048, DM = 640, NQKV = 1920, DFF = 1536;
constexpr unsigned WS_QKV = 0;
constexpr unsigned WS_ACT = WS_QKV + 120 * 64 * 1024;
constexpr unsigned WS_WT = WS_ACT + 32 * 68 * 4096;
constexpr unsigned WS_G = WS_WT + 8 * 68 * 5120;
constexpr unsigned WS_FLAG = WS_G + 8192;
constexpr unsigned WS_END = WS_FLAG + 4096;
constexpr unsigned WS_STAMP = WS_END;

#define MFMA(a, b, c) __builtin_amdgcn_mfma_f32_16x16x32_bf16((a), (b), (c), 0, 0, 0)
#define SBAR() __builtin_amdgcn_sched_barrier(0)

DI unsigned pk2(float a, float b) { f32x2 v = {a, b}; return __builtin_bit_cast(unsigned, __builtin_convertvector(v, bf16x2)); }
DI float bfround(float a) { return __uint_as_float(pk2(a, 0.f) << 16); }
DI u32x4 make_srd(const void* p, unsigned bytes) {
    uint64_t a = (uint64_t)p; u32x4 r = {(unsigned)a, (unsigned)(a >> 32) & 0xffffu, bytes, 0x00020000u}; return r; }
DI void dma16(u32x4 srd, unsigned voff, unsigned soff, unsigned ldsaddr) {
    asm volatile("s_mov_b32 m0, %0\n\ts_nop 0\n\tbuffer_load_dwordx4 %1, %2, %3 offen lds" :: "s"(ldsaddr), "v"(voff), "s"(srd), "s"(soff) : "memory");
}
typedef short v4i16_t __attribute__((ext_vector_type(4)));
DI u32x2 trread(lptr p) { return __builtin_bit_cast(u32x2, __builtin_amdgcn_ds_read_tr16_b64_v4i16((LAS v4i16_t*)p)); }
DI bf16x8 cat(u32x2 a, u32x2 b) { u32x4 v = {a[0], a[1], b[0], b[1]}; return __builtin_bit_cast(bf16x8, v); }
DI bf16x8 ldsr128(lptr p) { return *(LAS bf16x8*)p; }
DI u32x2 ldsr64(lptr p) { return *(LAS u32x2*)p; }

#define STAMP(ws, idx) do {} while (0)

constexpr int ST_X = 0, ST_WQ = 16384, ST_WH = 32768, ST_SZ = 45056;
constexpr int K1_RT = 3 * ST_SZ;
constexpr int K1_GW = K1_RT + 12288;
constexpr int K1_LDS = K1_GW + 2048;

struct K1Dma {
    u32x4 sx, sq, sh;
    unsigned vx0, vx1, vq, vh0, vh1;
    unsigned lds0; int wid; bool hasq;
};
template <int NWH>
DI void k1_issue(const K1Dma& d, int s, int buf) {
    const unsigned b = d.lds0 + buf * ST_SZ;
    dma16(d.sx, d.vx0, s * 128, b + ST_X + d.wid * 2048);
    dma16(d.sx, d.vx1, s * 128, b + ST_X + d.wid * 2048 + 1024);
    dma16(d.sq, d.vq, s * (64 * 3840), b + ST_WQ + d.wid * 2048);
    dma16(d.sq, d.vq, s * (64 * 3840) + 4 * 3840, b + ST_WQ + d.wid * 2048 + 1024);
    if (NWH == 1) dma16(d.sh, d.vh0, s * (64 * 3072), b + ST_WH + d.wid * 1024);
    else { dma16(d.sh, d.vh0, s * (64 * 3072), b + ST_WH + (2 * d.wid - 4) * 1024); dma16(d.sh, d.vh1, s * (64 * 3072), b + ST_WH + (2 * d.wid - 3) * 1024); }
}
template <int N> DI void wait_bar() {
    if (N == 0) asm volatile("s_waitcnt vmcnt(0) lgkmcnt(0)\n\ts_barrier" ::: "memory");
    if (N == 5) asm volatile("s_waitcnt vmcnt(5) lgkmcnt(0)\n\ts_barrier" ::: "memory");
    if (N == 6) asm volatile("s_waitcnt vmcnt(6) lgkmcnt(0)\n\ts_barrier" ::: "memory");
}

template <int ROLE> struct RoleCfg { static constexpr int NT = ROLE <= 1 ? 4 : 2, NS = ROLE <= 1 ? 4 : (ROLE == 2 ? 6 : 1), NWH = ROLE == 2 ? 2 : 1; };

template <int ROLE>
DI void k1_read(lptr sm, int buf, int s, int hh, const unsigned (&bx)[2], const unsigned (&bw)[4], bf16x8 (&xf)[RoleCfg<ROLE>::NT], bf16x8 (&wf)[RoleCfg<ROLE>::NS]) {
    constexpr int NT = RoleCfg<ROLE>::NT, NS = RoleCfg<ROLE>::NS;
    lptr st = sm + buf * ST_SZ;
#pragma unroll
    for (int t = 0; t < NT; ++t) xf[t] = ldsr128(st + bx[hh] + t * 2048);
    if (ROLE <= 1) {
#pragma unroll
        for (int u = 0; u < NS; ++u) wf[u] = cat(trread(st + bw[u] + hh * 8192), trread(st + bw[u] + hh * 8192 + 1024));
    } else if (ROLE == 2) {
#pragma unroll
        for (int u = 0; u < NS; ++u) wf[u] = cat(trread(st + bw[u & 1] + (u >> 1) * 64 + hh * 6144), trread(st + bw[u & 1] + (u >> 1) * 64 + hh * 6144 + 768));
    } else {
        wf[0] = ldsr128(sm + bw[0] + s * 128 + hh * 64);
    }
}
template <int ROLE>
DI void k1_mma(const bf16x8 (&xf)[RoleCfg<ROLE>::NT], const bf16x8 (&wf)[RoleCfg<ROLE>::NS], f32x4 (&acc)[RoleCfg<ROLE>::NT][RoleCfg<ROLE>::NS]) {
    constexpr int NT = RoleCfg<ROLE>::NT, NS = RoleCfg<ROLE>::NS;
#pragma unroll
    for (int u = 0; u < NS; ++u)
#pragma unroll
        for (int t = 0; t < NT; ++t) acc[t][u] = ROLE == 1 ? MFMA(xf[t], wf[u], acc[t][u]) : MFMA(wf[u], xf[t], acc[t][u]);
}

template <int ROLE>
DI void k1_loop(lptr sm, const K1Dma& d, const unsigned (&bx)[2], const unsigned (&bw)[4], f32x4 (&acc)[RoleCfg<ROLE>::NT][RoleCfg<ROLE>::NS]) {
    constexpr int NT = RoleCfg<ROLE>::NT, NS = RoleCfg<ROLE>::NS, NWH = RoleCfg<ROLE>::NWH, ND = 4 + NWH;
    bf16x8 xa[NT], wa[NS], xb[NT], wb[NS];
#pragma unroll
    for (int t = 0; t < NT; ++t)
#pragma unroll
        for (int u = 0; u < NS; ++u) acc[t][u] = (f32x4){0.f, 0.f, 0.f, 0.f};
    k1_issue<NWH>(d, 0, 0);
    k1_issue<NWH>(d, 1, 1);
    wait_bar<ND>();
    k1_issue<NWH>(d, 2, 2);
    k1_read<ROLE>(sm, 0, 0, 0, bx, bw, xa, wa);
    k1_read<ROLE>(sm, 0, 0, 1, bx, bw, xb, wb);
    k1_mma<ROLE>(xa, wa, acc);
    int buf = 1, nbuf = 0;
    for (int s = 1; s < 10; ++s) {
        if (s < 9) wait_bar<ND>(); else wait_bar<0>();
        if (s < 8) k1_issue<NWH>(d, s + 2, nbuf);
        k1_read<ROLE>(sm, buf, s, 0, bx, bw, xa, wa);
        SBAR();
        k1_mma<ROLE>(xb, wb, acc);
        SBAR();
        k1_read<ROLE>(sm, buf, s, 1, bx, bw, xb, wb);
        SBAR();
        k1_mma<ROLE>(xa, wa, acc);
        SBAR();
        nbuf = buf; buf = buf == 2 ? 0 : buf + 1;
    }
    k1_mma<ROLE>(xb, wb, acc);
}

__global__ __launch_bounds__(512, 2) void k1_kernel(const u16* __restrict__ x, const u16* __restrict__ wqkv, const u16* __restrict__ w1,
                                                     const u16* __restrict__ w2, const u16* __restrict__ wo, const u16* __restrict__ gw, char* __restrict__ ws) {
    __shared__ __attribute__((aligned(16))) char smem[K1_LDS];
    lptr sm = (lptr)smem;
    const int lane = threadIdx.x & 63;
    const int wid = __builtin_amdgcn_readfirstlane(threadIdx.x >> 6);
    const int r = lane & 15, g = lane >> 4;
    const int xcd = blockIdx.x & 7, idx = blockIdx.x >> 3;
    const int rb = (xcd & 1) * 8 + (idx & 7), cg = (xcd >> 1) * 4 + (idx >> 3);
    STAMP(ws, 0);
    K1Dma d;
    d.lds0 = (unsigned)(uintptr_t)sm; d.wid = wid; d.hasq = cg < 15;
    d.sx = make_srd(x, SEQ * DM * 2); d.sq = make_srd(wqkv, cg < 15 ? DM * NQKV * 2 : 0); d.sh = make_srd(w1, DM * DFF * 2);
    {
        int t0 = 16 * wid + (lane >> 3), t1 = t0 + 8;
        d.vx0 = (rb * 128 + t0) * 1280 + (((lane & 7) ^ ((t0 >> 1) & 7)) << 4);
        d.vx1 = (rb * 128 + t1) * 1280 + (((lane & 7) ^ ((t1 >> 1) & 7)) << 4);
        int row = 8 * wid + (lane >> 4); int h = (row & 3) | (((row >> 3) & 1) << 2);
        d.vq = row * 3840 + cg * 256 + (((lane & 15) ^ (h << 1)) << 4);
        int i0 = wid < 4 ? wid : 2 * wid - 4;
        int id0 = 64 * i0 + lane, id1 = id0 + 64;
        int r0 = id0 / 12, c0 = id0 - r0 * 12, r1 = id1 / 12, c1 = id1 - r1 * 12;
        d.vh0 = r0 * 3072 + cg * 192 + ((c0 ^ (((r0 >> 3) & 1) << 1)) << 4);
        d.vh1 = r1 * 3072 + cg * 192 + ((c1 ^ (((r1 >> 3) & 1) << 1)) << 4);
    }
    asm volatile("s_nop 4" ::: "memory");
    const int q4 = r >> 2, p4 = r & 3;
    unsigned bx[2], bw[4];
    const int blk = blockIdx.x + 256 * (wid - 4);
    const bool do_rt = wid >= 4 && wid < 7 && blk < 680;
    const int rks = blk / 10, rcl = blk - rks * 10;
    if (do_rt) {
        u32x4 srd = rks < 48 ? make_srd(w2, DFF * DM * 2) : make_srd(wo, DM * DM * 2);
#pragma unroll
        for (int i = 0; i < 4; ++i) {
            int rho = 8 * i + (lane >> 3), hf = rho >> 4, gg = (rho >> 2) & 3, jj = rho & 3;
            int row = rks < 48 ? 32 * rks + rho : (rks < 64 ? 40 * (rks - 48) + 20 * hf + 4 * gg + jj : 20 * (8 * (rks - 64) + 4 * hf + jj) + 16 + gg);
            dma16(srd, row * 1280 + rcl * 128 + (lane & 7) * 16, 0, d.lds0 + K1_RT + (wid - 4) * 4096 + i * 1024);
        }
    }
    if (blockIdx.x == 0 && wid == 7) {
#pragma unroll
        for (int i = 0; i < 4; ++i) *(u32x4*)(ws + WS_FLAG + i * 1024 + lane * 16) = (u32x4){0u, 0u, 0u, 0u};
    }
    if (wid < 4) {
        if (cg < 15) {
            const int th = wid & 1, cp = wid >> 1;
            bx[0] = ST_X + th * 8192 + r * 128 + ((g ^ (r >> 1)) << 4); bx[1] = bx[0] ^ 64;
            const int h = q4 | ((g & 1) << 2);
#pragma unroll
            for (int u = 0; u < 4; ++u) bw[u] = ST_WQ + (8 * g + q4) * 256 + (((4 * cp + u) ^ h) << 5) + 8 * p4;
            f32x4 acc[4][4];
            if (cg >= 10) k1_loop<1>(sm, d, bx, bw, acc); else k1_loop<0>(sm, d, bx, bw, acc);
            STAMP(ws, 1);
            const float sc = cg < 5 ? 0.22360679774997896f * 1.4426950408889634f : 1.0f;
            char* dst = ws + WS_QKV + (size_t)((8 * cg + 4 * cp) * 64 + 4 * rb + 2 * th) * 1024;
            const bool isk = cg >= 5 && cg < 10;
#pragma unroll
            for (int u = 0; u < 4; ++u)
#pragma unroll
                for (int pp = 0; pp < 2; ++pp) {
                    f32x4 a = acc[2 * pp][u] * sc, b = acc[2 * pp + 1][u] * sc;
                    u32x4 v = {pk2(a[0], a[1]), pk2(a[2], a[3]), pk2(b[0], b[1]), pk2(b[2], b[3])};
                    char* p = dst + u * 65536 + pp * 1024;
                    if (isk) { *(u32x2*)(p + lane * 8) = (u32x2){v[0], v[1]}; *(u32x2*)(p + 512 + lane * 8) = (u32x2){v[2], v[3]}; }
                    else *(u32x4*)(p + lane * 16) = v;
                }
        } else {
            if (wid < 2) dma16(make_srd(gw, 1280), wid * 1024 + lane * 16, 0, d.lds0 + K1_GW + wid * 1024);
            bx[0] = ST_X + wid * 4096 + r * 128 + ((g ^ (r >> 1)) << 4); bx[1] = bx[0] ^ 64;
            bw[0] = K1_GW + g * 16; bw[1] = bw[2] = bw[3] = 0;
            f32x4 acc[2][1];
            k1_loop<3>(sm, d, bx, bw, acc);
            STAMP(ws, 1);
#pragma unroll
            for (int t = 0; t < 2; ++t) {
                float z = bfround(acc[t][0][0]);
                float gt = bfround(1.0f / (1.0f + __expf(-z)));
                if (g == 0) ((float*)(ws + WS_G))[rb * 128 + wid * 32 + t * 16 + r] = gt;
            }
        }
    } else {
        const int tq = wid - 4;
        bx[0] = ST_X + tq * 4096 + r * 128 + ((g ^ (r >> 1)) << 4); bx[1] = bx[0] ^ 64;
        bw[0] = ST_WH + (8 * g + q4) * 192 + ((0 ^ (g & 1)) << 5) + 8 * p4;
        bw[1] = ST_WH + (8 * g + q4) * 192 + ((1 ^ (g & 1)) << 5) + 8 * p4; bw[2] = bw[3] = 0;
        f32x4 acc[2][6];
        k1_loop<2>(sm, d, bx, bw, acc);
        STAMP(ws, 1);
#pragma unroll
        for (int t = 0; t < 2; ++t) {
            const int tt = 8 * rb + 2 * tq + t;
            char* dst = ws + WS_ACT + (size_t)(((tt >> 2) * 68 + 3 * cg) * 4 + (tt & 3)) * 1024 + lane * 16;
#pragma unroll
            for (int q = 0; q < 3; ++q) {
                f32x4 a = acc[t][2 * q], b = acc[t][2 * q + 1];
                u32x4 v = {pk2(fmaxf(a[0], 0.f), fmaxf(a[1], 0.f)), pk2(fmaxf(a[2], 0.f), fmaxf(a[3], 0.f)), pk2(fmaxf(b[0], 0.f), fmaxf(b[1], 0.f)), pk2(fmaxf(b[2], 0.f), fmaxf(b[3], 0.f))};
                *(u32x4*)(dst + q * 4096) = v;
            }
        }
        if (do_rt) {
            lptr sc = sm + K1_RT + tq * 4096 + (4 * g + q4) * 128 + 8 * p4;
#pragma unroll
            for (int c4 = 0; c4 < 4; ++c4) {
                u32x2 a = trread(sc + c4 * 32), b = trread(sc + c4 * 32 + 2048);
                const int n16 = 4 * rcl + c4, ct = n16 / 5, nt = n16 - ct * 5;
                *(u32x4*)(ws + WS_WT + (size_t)((ct * 68 + rks) * 5 + nt) * 1024 + lane * 16) = (u32x4){a[0], a[1], b[0], b[1]};
            }
        }
    }
    STAMP(ws, 2);
}

constexpr int K2_K = 0, K2_V = 36864, K2_RED = 73728, K2_LDS = K2_RED + 61440;

DI float max3f(float a, float b, float c) { return fmaxf(fmaxf(a, b), c); }
DI float xmax16_32(float m) {
    auto a = __builtin_amdgcn_permlane16_swap(__float_as_uint(m), __float_as_uint(m), false, false);
    m = fmaxf(__uint_as_float(a[0]), __uint_as_float(a[1]));
    auto b = __builtin_amdgcn_permlane32_swap(__float_as_uint(m), __float_as_uint(m), false, false);
    return fmaxf(__uint_as_float(b[0]), __uint_as_float(b[1]));
}

template <int AUX> DI bf16x8 gload(__amdgpu_buffer_rsrc_t rs, unsigned voff, unsigned soff) {
    return __builtin_bit_cast(bf16x8, __builtin_amdgcn_raw_buffer_load_b128(rs, voff, soff, AUX));
}

__global__ __launch_bounds__(512, 2) void k2_kernel(char* __restrict__ ws, u16* __restrict__ out) {
    __shared__ __attribute__((aligned(16))) char smem[K2_LDS];
    lptr sm = (lptr)smem;
    const int lane = threadIdx.x & 63;
    const int wid = __builtin_amdgcn_readfirstlane(threadIdx.x >> 6);
    const int r = lane & 15, g = lane >> 4;
    const int xcd = blockIdx.x & 7, idx = blockIdx.x >> 3;
    const int hp = 2 * xcd + (idx & 1), rb = idx >> 1;
    const int rt = (xcd >> 1) * 8 + (idx & 7), ct = (xcd & 1) * 4 + (idx >> 3);
    const unsigned lds0 = (unsigned)(uintptr_t)sm;
    STAMP(ws, 3);
    const u32x4 sws = make_srd(ws, WS_END);
    const __amdgpu_buffer_rsrc_t rws = __builtin_amdgcn_make_buffer_rsrc(ws, 0, WS_END, 0x00020000);
    const int sb = (5 * hp) >> 1;
    const int odd = hp & 1;
    asm volatile("s_nop 4" ::: "memory");
    {
        const int fo_slab = odd ? 0 : 2, fo_r = odd ? 0 : 15;
        if (wid == 0 && lane < 48) *(LAS u32x4*)(sm + K2_V + fo_slab * 12288 + (lane >> 2) * 1024 + (fo_r + 16 * (lane & 3)) * 16) = (u32x4){0x3f803f80u, 0x3f803f80u, 0x3f803f80u, 0x3f803f80u};
#pragma unroll
        for (int i = 0; i < 9; ++i) {
            const int id = wid * 9 + i, kv = id / 36, sl = (id - kv * 36) / 12, tp = id - kv * 36 - sl * 12;
            int gtp = 4 * rb - 8 + tp; gtp = gtp < 0 ? 0 : gtp;
            const unsigned so = WS_QKV + ((40 + 40 * kv + sb + sl) * 64 + gtp) * 1024;
            const bool skip = kv == 1 && sl == fo_slab && r == fo_r;
            if (!skip) dma16(sws, lane * 16, so, lds0 + id * 1024);
        }
    }
    if (wid < 4) {
        u32x4 qr[3];
#pragma unroll
        for (int s = 0; s < 3; ++s) qr[s] = *(const u32x4*)(ws + WS_QKV + (size_t)((sb + s) * 64 + 4 * rb + wid) * 1024 + lane * 16);
        float gate[2];
#pragma unroll
        for (int e = 0; e < 2; ++e) gate[e] = ((const float*)(ws + WS_G))[rb * 128 + wid * 32 + e * 16 + r];
        asm volatile("s_waitcnt vmcnt(0) lgkmcnt(0)\n\ts_barrier" ::: "memory");
        STAMP(ws, 4);
        const int tfirst = 16 - 8 * rb - 2 * wid;
        unsigned om[2][2], orem[2];
        const f32x4 zero4 = {0.f, 0.f, 0.f, 0.f};
        const float NEG = -1e30f;
#pragma unroll 1
        for (int hh = 0; hh < 2; ++hh) {
            const int fb = odd * 8 + 20 * hh;
            const u32x4 qa = hh ? qr[1] : qr[0], qb = hh ? qr[2] : qr[1];
            const bool va = 16 * hh + 4 * g >= fb, vb = 16 * hh + 16 + 4 * g < fb + 20;
            bf16x8 qf[2];
#pragma unroll
            for (int e = 0; e < 2; ++e) { u32x4 v = {va ? qa[2 * e] : 0u, va ? qa[2 * e + 1] : 0u, vb ? qb[2 * e] : 0u, vb ? qb[2 * e + 1] : 0u}; qf[e] = __builtin_bit_cast(bf16x8, v); }
            lptr kp = sm + K2_K + hh * 12288 + wid * 1024 + lane * 8;
            f32x4 s0[18], s1[18];
#pragma unroll
            for (int t = 0; t < 18; ++t) {
                bf16x8 kf = cat(ldsr64(kp + t * 512), ldsr64(kp + 12288 + t * 512));
                if (t < 17) s0[t] = MFMA(kf, qf[0], zero4);
                if (t > 0) s1[t] = MFMA(kf, qf[1], zero4);
            }
#pragma unroll
            for (int i = 0; i < 4; ++i) {
                const bool le = 4 * g + i <= r;
                s0[16][i] = le ? s0[16][i] : NEG; s1[17][i] = le ? s1[17][i] : NEG;
                s0[0][i] = le ? NEG : s0[0][i]; s1[1][i] = le ? NEG : s1[1][i];
            }
            if (tfirst > 0) {
#pragma unroll
                for (int t = 0; t < 16; ++t) if (t < tfirst) { s0[t] = (f32x4){NEG, NEG, NEG, NEG}; if (t > 0) s1[t] = (f32x4){NEG, NEG, NEG, NEG}; }
            }
            float m0 = fmaxf(s0[0][0], s0[0][1]), m1 = fmaxf(s1[17][0], s1[17][1]);
            m0 = max3f(m0, s0[0][2], s0[0][3]); m1 = max3f(m1, s1[17][2], s1[17][3]);
#pragma unroll
            for (int t = 1; t < 17; ++t) {
                m0 = max3f(m0, s0[t][0], s0[t][1]); m0 = max3f(m0, s0[t][2], s0[t][3]);
                m1 = max3f(m1, s1[t][0], s1[t][1]); m1 = max3f(m1, s1[t][2], s1[t][3]);
            }
            m0 = xmax16_32(m0); m1 = xmax16_32(m1);
            u32x2 p0[18], p1[18];
#pragma unroll
            for (int t = 0; t < 18; ++t) {
                if (t < 17) p0[t] = (u32x2){pk2(__builtin_amdgcn_exp2f(s0[t][0] - m0), __builtin_amdgcn_exp2f(s0[t][1] - m0)), pk2(__builtin_amdgcn_exp2f(s0[t][2] - m0), __builtin_amdgcn_exp2f(s0[t][3] - m0))};
                else p0[t] = (u32x2){0u, 0u};
                if (t > 0) p1[t] = (u32x2){pk2(__builtin_amdgcn_exp2f(s1[t][0] - m1), __builtin_amdgcn_exp2f(s1[t][1] - m1)), pk2(__builtin_amdgcn_exp2f(s1[t][2] - m1), __builtin_amdgcn_exp2f(s1[t][3] - m1))};
                else p1[t] = (u32x2){0u, 0u};
            }
            const int f0 = fb + r, f1 = (r & 3) == 0 ? fb + 16 + (r >> 2) : (odd ? 0 : 47);
            lptr v0p = sm + K2_V + wid * 1024 + (f0 >> 4) * 12288 + ((f0 & 15) + 16 * g) * 16;
            lptr v1p = sm + K2_V + wid * 1024 + (f1 >> 4) * 12288 + ((f1 & 15) + 16 * g) * 16;
            f32x4 o00 = zero4, o01 = zero4, o10 = zero4, o11 = zero4;
#pragma unroll
            for (int p = 0; p < 9; ++p) {
                bf16x8 va0 = ldsr128(v0p + p * 1024), va1 = ldsr128(v1p + p * 1024);
                bf16x8 pb0 = cat(p0[2 * p], p0[2 * p + 1]), pb1 = cat(p1[2 * p], p1[2 * p + 1]);
                o00 = MFMA(va0, pb0, o00); o01 = MFMA(va1, pb0, o01);
                o10 = MFMA(va0, pb1, o10); o11 = MFMA(va1, pb1, o11);
            }
            const float c0 = gate[0] * __builtin_amdgcn_rcpf(o01[1]), c1 = gate[1] * __builtin_amdgcn_rcpf(o11[1]);
            const unsigned a0 = pk2(o00[0] * c0, o00[1] * c0), a1 = pk2(o00[2] * c0, o00[3] * c0);
            const unsigned b0 = pk2(o10[0] * c1, o10[1] * c1), b1 = pk2(o10[2] * c1, o10[3] * c1);
            const unsigned rr = pk2(o01[0] * c0, o11[0] * c1);
            if (hh == 0) { om[0][0] = a0; om[0][1] = a1; om[1][0] = b0; om[1][1] = b1; orem[0] = rr; }
            else {
                const int tt = 8 * rb + 2 * wid;
                const unsigned base = WS_ACT + ((tt >> 2) * 68 * 4 + (tt & 3)) * 1024;
                __builtin_amdgcn_raw_buffer_store_b128((u32x4){om[0][0], om[0][1], a0, a1}, rws, lane * 16, base + (48 + hp) * 4096, 16);
                __builtin_amdgcn_raw_buffer_store_b128((u32x4){om[1][0], om[1][1], b0, b1}, rws, lane * 16, base + (48 + hp) * 4096 + 1024, 16);
                const unsigned r0 = (orem[0] & 0xffffu) | (rr << 16), r1 = (orem[0] >> 16) | (rr & 0xffff0000u);
                __builtin_amdgcn_raw_buffer_store_b32(r0, rws, lane * 16 + (hp & 3) * 4, base + (64 + (hp >> 2)) * 4096, 16);
                __builtin_amdgcn_raw_buffer_store_b32(r1, rws, lane * 16 + (hp & 3) * 4, base + (64 + (hp >> 2)) * 4096 + 1024, 16);
            }
        }
        STAMP(ws, 5);
        asm volatile("s_waitcnt vmcnt(0)" ::: "memory");
        if (lane == 0) __hip_atomic_store((GAS unsigned*)(ws + WS_FLAG) + (2 * rb + (wid >> 1)) * 32 + hp * 2 + (wid & 1), 1u, __ATOMIC_RELAXED, __HIP_MEMORY_SCOPE_AGENT);
        STAMP(ws, 6);
        return;
    }
    const int mw = wid - 4;
    const unsigned abase = WS_ACT + (rt * 68 + mw) * 4096, wbase = WS_WT + (ct * 68 + mw) * 5120;
    const unsigned vo = lane * 16;
    bf16x8 a0[4], w0[5], a1[4], w1f[5];
    f32x4 acc[4][5];
#pragma unroll
    for (int t = 0; t < 4; ++t)
#pragma unroll
        for (int n = 0; n < 5; ++n) acc[t][n] = (f32x4){0.f, 0.f, 0.f, 0.f};
#define LDW(W, i) _Pragma("unroll") for (int n = 0; n < 5; ++n) W[n] = gload<0>(rws, vo + n * 1024, wbase + (i) * 20480)
#define LDA(A, i, AUX) _Pragma("unroll") for (int t = 0; t < 4; ++t) A[t] = gload<AUX>(rws, vo + t * 1024, abase + (i) * 16384)
#define MMA(A, W) _Pragma("unroll") for (int n = 0; n < 5; ++n) _Pragma("unroll") for (int t = 0; t < 4; ++t) acc[t][n] = MFMA(W[n], A[t], acc[t][n])
    LDA(a0, 0, 0); LDW(w0, 0); LDA(a1, 1, 0); LDW(w1f, 1);
    float gate[4];
#pragma unroll
    for (int t = 0; t < 4; ++t) gate[t] = ((const float*)(ws + WS_G))[rt * 64 + t * 16 + r];
    asm volatile("s_waitcnt vmcnt(22)\n\ts_barrier" ::: "memory");
    STAMP(ws, 4);
#pragma unroll 1
    for (int i = 0; i < 10; i += 2) {
        MMA(a0, w0); SBAR();
        LDA(a0, i + 2, 0); LDW(w0, i + 2); SBAR();
        MMA(a1, w1f); SBAR();
        LDA(a1, i + 3, 0); LDW(w1f, i + 3); SBAR();
    }
    MMA(a0, w0); SBAR();
    LDW(w0, 12); SBAR();
    MMA(a1, w1f); SBAR();
    LDW(w1f, 13); SBAR();
#pragma unroll
    for (int t = 0; t < 4; ++t) {
        const float og = bfround(1.0f - gate[t]);
#pragma unroll
        for (int n = 0; n < 5; ++n) acc[t][n] *= og;
    }
    STAMP(ws, 5);
    {
        GAS unsigned* fl = (GAS unsigned*)(ws + WS_FLAG) + rt * 32 + (lane & 31);
        for (int spin = 0; spin < (1 << 20); ++spin) {
            unsigned v = __hip_atomic_load(fl, __ATOMIC_RELAXED, __HIP_MEMORY_SCOPE_AGENT);
            if (__all(v == 1u)) break;
            __builtin_amdgcn_s_sleep(1);
        }
        __builtin_amdgcn_fence(__ATOMIC_ACQUIRE, "wavefront");
    }
    STAMP(ws, 6);
    bf16x8 a2[4], a3[4], a4[4], w2f[5];
    LDA(a0, 12, 16); LDA(a1, 13, 16); LDA(a2, 14, 16); LDW(w2f, 14); LDA(a3, 15, 16); LDA(a4, 16, 16); SBAR();
    MMA(a0, w0); SBAR();
    LDW(w0, 15); SBAR();
    MMA(a1, w1f); SBAR();
    LDW(w1f, 16); SBAR();
    MMA(a2, w2f); SBAR();
    MMA(a3, w0); SBAR();
    MMA(a4, w1f); SBAR();
    STAMP(ws, 7);
#pragma unroll
    for (int t = 0; t < 4; ++t) {
        if (t != mw) {
            const int slot = mw > t ? mw - 1 : mw;
#pragma unroll
            for (int n = 0; n < 5; ++n) *(LAS f32x4*)(sm + K2_RED + ((t * 3 + slot) * 5 + n) * 1024 + lane * 16) = acc[t][n];
        }
    }
    asm volatile("s_waitcnt lgkmcnt(0)\n\ts_barrier" ::: "memory");
    f32x4 fin[5];
#pragma unroll
    for (int n = 0; n < 5; ++n) fin[n] = mw == 0 ? acc[0][n] : mw == 1 ? acc[1][n] : mw == 2 ? acc[2][n] : acc[3][n];
#pragma unroll
    for (int s = 0; s < 3; ++s)
#pragma unroll
        for (int n = 0; n < 5; ++n) fin[n] += *(LAS f32x4*)(sm + K2_RED + ((mw * 3 + s) * 5 + n) * 1024 + lane * 16);
    u16* op = out + (size_t)(rt * 64 + mw * 16 + r) * DM + ct * 80 + 4 * g;
#pragma unroll
    for (int n = 0; n < 5; ++n) *(u32x2*)(op + n * 16) = (u32x2){pk2(fin[n][0], fin[n][1]), pk2(fin[n][2], fin[n][3])};
    STAMP(ws, 8);
}

extern "C" void kernel_launch(void* const* d_in, const int* in_sizes, int n_in, void* d_out, int out_size, void* d_ws, size_t ws_size, hipStream_t stream) {
    const u16* x = (const u16*)d_in[0];
    const u16* wqkv = (const u16*)d_in[1];
    const u16* wo = (const u16*)d_in[2];
    const u16* w1 = (const u16*)d_in[4];
    const u16* w2 = (const u16*)d_in[5];
    const u16* gw = (const u16*)d_in[6];
    k1_kernel<<<256, 512, 0, stream>>>(x, wqkv, w1, w2, wo, gw, (char*)d_ws);
    k2_kernel<<<256, 512, 0, stream>>>((char*)d_ws, (u16*)d_out);
}
```

```cpp
#include <hip/hip_runtime.h>
#include <stdint.h>

typedef unsigned short u16;
typedef __attribute__((ext_vector_type(8))) __bf16 bf16x8;
typedef __attribute__((ext_vector_type(2))) __bf16 bf16x2;
typedef __attribute__((ext_vector_type(4))) float f32x4;
typedef __attribute__((ext_vector_type(2))) float f32x2;
typedef __attribute__((ext_vector_type(16))) float f32x16;
typedef __attribute__((ext_vector_type(4))) unsigned u32x4;
typedef __attribute__((ext_vector_type(2))) unsigned u32x2;
typedef __attribute__((ext_vector_type(16))) unsigned u32x16;
#define LAS __attribute__((address_space(3)))
#define GAS __attribute__((address_space(1)))
#define DI __device__ __forceinline__
typedef LAS char* lptr;
typedef __amdgpu_buffer_rsrc_t rsrc_t;

constexpr int SEQ = 2048, DM = 640, NQKV = 1920, DFF = 1536;
constexpr unsigned WS_QKV = 0;
constexpr unsigned WS_ACT = WS_QKV + 120 * 64 * 1024;
constexpr unsigned WS_WT = WS_ACT + 32 * 68 * 4096;
constexpr unsigned WS_G = WS_WT + 8 * 68 * 5120;
constexpr unsigned WS_FQ = WS_G + 8192;
constexpr unsigned WS_FH = WS_FQ + 1024;
constexpr unsigned WS_FO = WS_FH + 1024;
constexpr unsigned WS_FW = WS_FO + 1024;
constexpr unsigned WS_E = WS_FW + 1024;
constexpr unsigned WS_END = WS_E + 256;
constexpr unsigned WS_STAMP = WS_END;
constexpr unsigned OOB = 0x7f000000u;
constexpr int SC1 = 16;

#define MFMA(a, b, c) __builtin_amdgcn_mfma_f32_16x16x32_bf16((a), (b), (c), 0, 0, 0)
#define MFMA32(a, b, c) __builtin_amdgcn_mfma_f32_32x32x16_bf16((a), (b), (c), 0, 0, 0)
#define SBAR() __builtin_amdgcn_sched_barrier(0)
#define OPAQUE(v) asm volatile("" : "+v"(v))

DI unsigned pk2(float a, float b) { f32x2 v = {a, b}; return __builtin_bit_cast(unsigned, __builtin_convertvector(v, bf16x2)); }
DI float bfround(float a) { return __uint_as_float(pk2(a, 0.f) << 16); }
DI u32x4 make_srd(const void* p, unsigned bytes) {
    uint64_t a = (uint64_t)p; u32x4 r = {(unsigned)a, (unsigned)(a >> 32) & 0xffffu, bytes, 0x00020000u}; return r; }
DI void dma16(u32x4 srd, unsigned voff, unsigned soff, unsigned ldsaddr) {
    asm volatile("s_mov_b32 m0, %0\n\ts_nop 0\n\tbuffer_load_dwordx4 %1, %2, %3 offen lds" :: "s"(ldsaddr), "v"(voff), "s"(srd), "s"(soff) : "memory");
}
typedef short v4i16_t __attribute__((ext_vector_type(4)));
DI u32x2 trread(unsigned a) { return __builtin_bit_cast(u32x2, __builtin_amdgcn_ds_read_tr16_b64_v4i16((LAS v4i16_t*)(uintptr_t)a)); }
DI bf16x8 cat(u32x2 a, u32x2 b) { u32x4 v = {a[0], a[1], b[0], b[1]}; return __builtin_bit_cast(bf16x8, v); }
DI bf16x8 ldsr128(unsigned a) { return *(LAS bf16x8*)(uintptr_t)a; }
DI u32x2 ldsr64(unsigned a) { return *(LAS u32x2*)(uintptr_t)a; }
template <int N> DI void wait_bar() { asm volatile("s_waitcnt vmcnt(%0)\n\ts_barrier" :: "n"(N) : "memory"); }
template <int N> DI void wait_bar_lgkm() { asm volatile("s_waitcnt vmcnt(%0) lgkmcnt(0)\n\ts_barrier" :: "n"(N) : "memory"); }
DI void st128(u32x4 v, u32x4 srd, unsigned voff, unsigned soff) { asm volatile("buffer_store_dwordx4 %0, %1, %2, %3 offen sc1\n\ts_nop 1" :: "v"(v), "v"(voff), "s"(srd), "s"(soff) : "memory"); }
DI void st32(unsigned v, rsrc_t rs, unsigned voff, unsigned soff) { __builtin_amdgcn_raw_buffer_store_b32(v, rs, voff, soff, SC1); }
DI bf16x8 ld128(rsrc_t rs, unsigned voff, unsigned soff) { return __builtin_bit_cast(bf16x8, __builtin_amdgcn_raw_buffer_load_b128(rs, voff, soff, SC1)); }
DI void poll(const char* ws, unsigned off, unsigned nonce) {
#pragma unroll 1
    for (int spin = 0; spin < (1 << 18); ++spin) {
        unsigned v;
        asm volatile("global_load_dword %0, %1, %2 sc1\n\ts_waitcnt vmcnt(0)" : "=v"(v) : "v"(off), "s"(ws) : "memory");
        if (__all(v == nonce)) break;
        __builtin_amdgcn_s_sleep(1);
    }
}
DI void poll4(const char* ws, unsigned off, unsigned nonce) {
#pragma unroll 1
    for (int spin = 0; spin < (1 << 18); ++spin) {
        u32x4 v;
        asm volatile("global_load_dwordx4 %0, %1, %2 sc1\n\ts_waitcnt vmcnt(0)" : "=v"(v) : "v"(off), "s"(ws) : "memory");
        if (__all(v[0] == nonce && v[1] == nonce && v[2] == nonce && v[3] == nonce)) break;
        __builtin_amdgcn_s_sleep(1);
    }
}

#define STAMP(idx) do {} while (0)
#define STAMP_FLUSH() do {} while (0)
#define CSTAMP(idx) do {} while (0)

constexpr int SLOT = 32768, LDS_BYTES = 5 * SLOT;
constexpr int sbase(int n) { return (n % 5) * SLOT; }
constexpr int L_RT = 122880;
constexpr int L_GW = 4 * SLOT + 24576;
constexpr int L_CNT = 0, L_Q = 1024, L_GT = 13312, L_V = 14336, L_RED = 53248, L_K = 126976;
#define SB12 56

struct Ctx {
    u32x4 sx, sq, sh, s2;
    rsrc_t rws; u32x4 sws;
    unsigned vx0, vx1, vq0, vq1, vh0, vh1, vh2, vrt, srt;
    unsigned bx, bw, bh, brt;
    unsigned lane16;
    int wid, rb, cg;
    unsigned nonce;
    unsigned rd0, rd1;
};
constexpr int nWait(int role, int ch, int m) {
    if (role != 2) return m == 10 ? 9 : 8;
    return m == 0 ? 8 : m <= 4 ? 10 : m == 5 ? 8 : m == 6 ? 6 : m == 7 ? 4 : m == 8 ? 6 : 8;
}
template <int ROLE, int CH> DI void bar_stage(int m) {
    if (m == 1) wait_bar<nWait(ROLE, CH, 1)>(); if (m == 2) wait_bar<nWait(ROLE, CH, 2)>(); if (m == 3) wait_bar<nWait(ROLE, CH, 3)>(); if (m == 4) wait_bar<nWait(ROLE, CH, 4)>();
    if (m == 5) wait_bar<nWait(ROLE, CH, 5)>(); if (m == 6) wait_bar<nWait(ROLE, CH, 6)>(); if (m == 7) wait_bar<nWait(ROLE, CH, 7)>(); if (m == 8) wait_bar<nWait(ROLE, CH, 8)>();
    if (m == 9) wait_bar_lgkm<nWait(ROLE, CH, 9)>(); if (m == 10) wait_bar<nWait(ROLE, CH, 10)>();
}
DI unsigned rt_doff(int rks, int n16) { const int ct = (n16 * 205) >> 10, nt = n16 - ct * 5; return WS_WT + ((ct * 68 + rks) * 5 + nt) * 1024; }

template <int ROLE> DI void issue_piece(const Ctx& c, int n, int i) {
    const unsigned b = sbase(n);
    if (n < 10) {
        if (i == 0) dma16(c.sx, c.vx0, n * 128, b + c.wid * 2048);
        if (i == 1) dma16(c.sx, c.vx1, n * 128, b + c.wid * 2048 + 1024);
        if (ROLE != 2) {
            if (i == 2) dma16(c.sq, c.vq0, n * (64 * 3840), b + 16384 + c.wid * 2048);
            if (i == 3) dma16(c.sq, c.vq0, n * (64 * 3840) + 4 * 3840, b + 16384 + c.wid * 2048 + 1024);
        } else if (n < 3) {
            const int l = n * 4 + (c.wid >> 1);
            const unsigned so = l < 10 ? l * 128 : OOB;
            if (i == 2) dma16(c.sq, c.vq0, so, b + 16384 + c.wid * 2048);
            if (i == 3) dma16(c.sq, c.vq0, so + 20 * 1280, b + 16384 + c.wid * 2048 + 1024);
        } else if (n == 3) {
            if (i == 2) dma16(c.sq, c.vq1, c.srt, b + 16384 + c.wid * 2048);
            if (i == 3) dma16(c.sq, c.vq1, c.srt + 80 * 1280, b + 16384 + c.wid * 2048 + 1024);
        }
    } else {
        const int t = (n - 10) * 4 + i;
        if (n == 12 && i == 0 && ROLE != 2) dma16(c.s2, c.vrt, c.srt, L_RT + c.wid * 1024);
        if (t < 12) dma16(c.sh, t % 3 == 0 ? c.vh0 : t % 3 == 1 ? c.vh1 : c.vh2, (t / 3) * (128 * 3072), t * 8192 + c.wid * 1024);
    }
}
DI bf16x8 rd_wq(unsigned bw, int J, int u) {
    const int k = 2 * J + (u >> 1);
    const unsigned a = bw ^ (unsigned)(((u & 1) << 6) | sbase(k >> 2));
    return cat(trread(a + (k & 3) * 4096), trread(a + (k & 3) * 4096 + 1024));
}
constexpr int KT = 28;
constexpr int p2k(int s) { return s < 3 * KT ? s / 3 : KT + (s - 3 * KT) % (40 - KT); }
constexpr int p2v(int s) { return s < 3 * KT ? s % 3 : (s - 3 * KT) / (40 - KT); }
DI bf16x8 rd_wh(unsigned bh, int s) {
    const int k = p2k(s), v = p2v(s);
    const unsigned a = bh + (k >= 20 ? 61440 : 0);
    const int o = (k % 20) * 3072 + v * 64;
    return cat(trread(a + o), trread(a + o + 768));
}
DI bf16x8 rd_x(unsigned bx, int k) { return ldsr128(bx ^ (unsigned)(((k & 3) << 5) | sbase(k >> 2))); }
DI void retile1(const Ctx& c, unsigned a, unsigned doff) {
    const u32x2 a0 = trread(a), b0 = trread(a + 2048);
    st128((u32x4){a0[0], a0[1], b0[0], b0[1]}, c.sws, c.lane16, doff);
}
DI u32x4 pk8(const f32x16& a, int i, int j) { return (u32x4){pk2(a[i], a[i + 1]), pk2(a[i + 2], a[i + 3]), pk2(a[j], a[j + 1]), pk2(a[j + 2], a[j + 3])}; }

DI void signal(unsigned cnt, int lane, unsigned nonce, rsrc_t rws, unsigned flag) {
    asm volatile("s_waitcnt vmcnt(0)" ::: "memory");
    if (lane == 0) {
        unsigned old;
        asm volatile("ds_add_rtn_u32 %0, %1, %2\n\ts_waitcnt lgkmcnt(0)" : "=v"(old) : "v"(cnt), "v"(1) : "memory");
        if (old == 3) st32(nonce, rws, 0, flag);
    }
}
DI void lds_count(unsigned cnt, int lane) { if (lane == 0) asm volatile("s_waitcnt lgkmcnt(0)\n\tds_add_u32 %0, %1" :: "v"(cnt), "v"(1) : "memory"); }
DI void lds_wait(unsigned cnt) {
#pragma unroll 1
    for (int spin = 0; spin < (1 << 20); ++spin) {
        unsigned v;
        asm volatile("ds_read_b32 %0, %1\n\ts_waitcnt lgkmcnt(0)" : "=v"(v) : "v"(cnt) : "memory");
        if (__builtin_amdgcn_readfirstlane(v) == 4) break;
    }
}
template <int I> struct IC { static constexpr int v = I; };
template <int A, int B, class F> DI void sfor(F&& f) { if constexpr (A < B) { f(IC<A>{}); sfor<A + 1, B>(f); } }

template <int ROLE, int CH>
DI void phase12(Ctx& c, char* ws) {
    constexpr int NW = 5;
    bf16x8 xf[40], wf[NW];
    f32x16 acc[2];
    const f32x16 zero16 = {0.f, 0.f, 0.f, 0.f, 0.f, 0.f, 0.f, 0.f, 0.f, 0.f, 0.f, 0.f, 0.f, 0.f, 0.f, 0.f};
    const int tq = c.wid & 3;
    const int lane = c.lane16 >> 4;
    issue_piece<ROLE>(c, 0, 2); issue_piece<ROLE>(c, 0, 3);
#pragma unroll
    for (int n = 1; n < 3; ++n)
#pragma unroll
        for (int i = 0; i < 4; ++i) issue_piece<ROLE>(c, n, i);
    unsigned e;
    asm volatile("s_load_dword %0, %1, %3\n\ts_waitcnt vmcnt(%2) lgkmcnt(0)\n\ts_barrier" : "=s"(e) : "s"(ws), "n"(nWait(ROLE, CH, 0)), "s"(WS_E) : "memory");
    c.nonce = e * 0x9E3779B1u + 0x7F4A7C15u;
    STAMP(1); CSTAMP(12);
    if (ROLE != 2) {
        xf[0] = rd_x(c.bx, 0); xf[1] = rd_x(c.bx, 1);
#pragma unroll
        for (int u = 0; u < 4; ++u) wf[u] = rd_wq(c.bw, 0, u);
        SBAR();
#pragma unroll
        for (int J = 0; J < 20; ++J) {
            OPAQUE(c.bx); OPAQUE(c.bw);
#pragma unroll
            for (int u = 0; u < 4; ++u) {
                const int k = 2 * J + (u >> 1), v = u & 1;
                const f32x16 ci = k == 0 ? zero16 : acc[v];
                acc[v] = ROLE == 1 ? MFMA32(xf[k], wf[u], ci) : MFMA32(wf[u], xf[k], ci);
                SBAR();
                if (u == 0 && (J & 1)) {
                    bar_stage<ROLE, CH>((J + 1) >> 1); STAMP(16 + ((J + 1) >> 1));
                }
                if (u < 2 && J < 19) xf[2 * J + 2 + u] = rd_x(c.bx, 2 * J + 2 + u);
                if (J < 19) wf[u] = rd_wq(c.bw, J + 1, u);
                else if (CH) { wf[u] = rd_wh(c.bh, u); if (u == 3) wf[4] = rd_wh(c.bh, 4); }
                if (J == 0) issue_piece<ROLE>(c, 3, u);
                else if (u >= 2) issue_piece<ROLE>(c, ((J + 1) >> 1) + 3, (u - 2) + ((J & 1) ? 0 : 2));
                SBAR();
            }
        }
    } else {
        f32x4 z0 = {0.f, 0.f, 0.f, 0.f}, z1 = z0;
        const unsigned ga = L_GW + (((lane ^ (lane >> 4)) & 1) ? 2048 : (lane >> 5) * 16);
#pragma unroll
        for (int m = 0; m <= 10; ++m) {
            bar_stage<2, CH>(m);
#pragma unroll
            for (int i = 0; i < 4; ++i) issue_piece<2>(c, m + 3, i);
            if (m < 10) {
                OPAQUE(c.bx);
#pragma unroll
                for (int i = 0; i < 4; ++i) xf[4 * m + i] = rd_x(c.bx, 4 * m + i);
                if (m < 4) {
                    const int l = m * 4 + (c.wid >> 1), id = c.rb * 3 + (c.wid >> 1);
                    const bool valid = m < 3 ? l < 10 : (c.wid >> 1) < 3 && id < 40;
                    const int rks = m < 3 ? 48 + c.rb : 64 + (id & 3), n16 = 4 * (m < 3 ? l : id >> 2) + (c.wid & 1) * 2;
                    retile1(c, c.bw + sbase(m), valid ? rt_doff(rks, n16) : OOB);
                    retile1(c, c.bw + sbase(m) + 32, valid ? rt_doff(rks, n16 + 1) : OOB);
                }
                if (CH == 0) {
#pragma unroll
                    for (int i = 0; i < 4; i += 2) { z0 = MFMA(ldsr128(ga + (4 * m + i) * 32), xf[4 * m + i], z0); z1 = MFMA(ldsr128(ga + (4 * m + i + 1) * 32), xf[4 * m + i + 1], z1); }
                } else asm volatile("s_waitcnt lgkmcnt(0)" ::: "memory");
            }
            SBAR();
        }
        if (CH) {
#pragma unroll
            for (int u = 0; u < NW; ++u) wf[u] = rd_wh(c.bh, u);
        } else {
            z0 += z1;
            const float z = bfround((lane & 16) ? z0[1] : z0[0]);
            const float gt = bfround(1.0f / (1.0f + __expf(-z)));
            st32(__float_as_uint(gt), c.rws, lane < 32 ? lane * 4 : OOB, WS_G + (c.rb * 128 + tq * 32) * 4);
        }
    }
    STAMP(2); CSTAMP(13);
    const unsigned so = WS_QKV + ((8 * c.cg + 4 * CH) * 64 + 4 * c.rb + tq) * 1024;
    const unsigned vst = ROLE == 0 ? c.lane16 : ((lane >> 4) & 1) * 65536 + (lane >> 5) * 256 + (lane & 15) * 16;
    constexpr unsigned sd = ROLE == 0 ? 65536 : 512;
    if (CH == 0) {
        if (ROLE != 2) {
#pragma unroll
            for (int v = 0; v < 2; ++v) { st128(pk8(acc[v], 0, 4), c.sws, vst, so + v * 131072); st128(pk8(acc[v], 8, 12), c.sws, vst, so + v * 131072 + sd); }
        }
        wait_bar<ROLE != 2 ? 8 : 5>();
        return;
    }
    f32x16 hc[3];
    u32x4 pq[4];
    unsigned cw;
    const int tt = 8 * c.rb + 2 * tq;
    const unsigned sa = WS_ACT + (((tt >> 2) * 68 + 3 * c.cg) * 4 + (tt & 3)) * 1024;
    const unsigned vo = ((lane >> 4) & 1) * 1024 + (lane >> 5) * 256 + (lane & 15) * 16;
    auto hstore = [&](int v, int half) __attribute__((always_inline)) {
        f32x16 a = hc[v];
#pragma unroll
        for (int i = 0; i < 4; ++i) { a[half * 4 + i] = fmaxf(a[half * 4 + i], 0.f); a[half * 4 + 8 + i] = fmaxf(a[half * 4 + 8 + i], 0.f); }
        st128(pk8(a, half * 4, half * 4 + 8), c.sws, vo, sa + v * 4096 + half * 512);
    };
    constexpr int SW = 3 * KT + (32 - KT) - NW;
    sfor<0, 120>([&](auto S) __attribute__((always_inline)) {
        constexpr int s = S.v, k = p2k(s), v = p2v(s);
        hc[v] = MFMA32(wf[s % NW], xf[k], k == 0 ? zero16 : hc[v]);
        SBAR();
        if (s == 10) { wait_bar<ROLE != 2 ? 8 : 4>(); STAMP(27); }
        if (s == SB12) { CSTAMP(14); wait_bar<0>(); CSTAMP(15); STAMP(28); }
        if (s == SW - 4) cw = *(volatile LAS unsigned*)(uintptr_t)(L_CNT + 16);
        if (s == SW) { if (__builtin_amdgcn_readfirstlane(cw) != 4) lds_wait(L_CNT + 16); }
        if (s + NW < 120) wf[s % NW] = rd_wh(c.bh, s + NW);
        if constexpr (s >= 3 * KT + (40 - KT) && (k == KT + 2 || k == KT + 5)) hstore(v - 1, k == KT + 5);
        if (ROLE != 2) {
            if constexpr (s < 2) { pq[2 * s] = pk8(acc[s], 0, 4); pq[2 * s + 1] = pk8(acc[s], 8, 12); }
            if constexpr (s >= 30 && s < 34) { OPAQUE(c.bx); xf[s + 6] = rd_x(c.bx, s + 6); }
            if constexpr (s < 8 && (s & 1)) { constexpr int q = s >> 1; st128(pq[q], c.sws, vst, so + (q >> 1) * 131072 + (q & 1) * sd); }
        }
        SBAR();
    });
    STAMP(3);
    hstore(2, 0); hstore(2, 1);
}

DI float max3f(float a, float b, float c) { return fmaxf(fmaxf(a, b), c); }
DI const void* mkptr(unsigned lo, unsigned hi) { return (const void*)(((uint64_t)hi << 32) | lo); }

__global__ __launch_bounds__(512, 2) void fused_kernel(const u16* x_, const u16* wqkv_, const u16* w1_, const u16* w2_, const u16* wo_, const u16* gw_, char* ws_, u16* out_) {
    __shared__ __attribute__((aligned(16))) char smem[LDS_BYTES];
    Ctx c;
    u32x16 ka;
    asm volatile("s_load_dwordx16 %0, %1, 0x0" : "=s"(ka) : "s"(__builtin_amdgcn_kernarg_segment_ptr()) : "memory");
    STAMP(0);
    const int lane = threadIdx.x & 63;
    const int wid = __builtin_amdgcn_readfirstlane(threadIdx.x >> 6);
    const int r = lane & 15, g = lane >> 4, q4 = r >> 2, p4 = r & 3;
    const int xcd = blockIdx.x & 7, idx = blockIdx.x >> 3;
    char* ws;
    {
        const int rb = (xcd & 1) * 8 + (idx & 7), cg = (xcd >> 1) * 4 + (idx >> 3);
        c.wid = wid; c.rb = rb; c.cg = cg; c.lane16 = lane * 16;
        const int t0 = 16 * wid + (lane >> 3), t1 = t0 + 8;
        c.vx0 = (rb * 128 + t0) * 1280 + (((lane & 7) ^ ((t0 >> 1) & 7)) << 4);
        c.vx1 = (rb * 128 + t1) * 1280 + (((lane & 7) ^ ((t1 >> 1) & 7)) << 4);
        asm volatile("s_waitcnt lgkmcnt(0)" : "+s"(ka), "+v"(c.vx0), "+v"(c.vx1) :: "memory");
        asm volatile("" :: "v"((LAS char*)smem) : "memory");
        c.sx = make_srd(mkptr(ka[0], ka[1]), SEQ * DM * 2);
        issue_piece<0>(c, 0, 0); issue_piece<0>(c, 0, 1);
        SBAR();
        ws = (char*)mkptr(ka[12], ka[13]);
        c.sq = cg < 15 ? make_srd(mkptr(ka[2], ka[3]), DM * NQKV * 2) : make_srd(mkptr(ka[8], ka[9]), DM * DM * 2);
        c.sh = make_srd(mkptr(ka[4], ka[5]), DM * DFF * 2); c.s2 = make_srd(mkptr(ka[6], ka[7]), DFF * DM * 2);
        c.sws = make_srd(ws, WS_END);
        c.rws = __builtin_amdgcn_make_buffer_rsrc(ws, 0, WS_END, 0x00020000);
        const int id0 = wid * 64 + lane, id1 = id0 + 512, id2 = id0 + 1024;
        const int r0 = id0 / 12, r1 = id1 / 12, r2 = id2 / 12;
        c.vh0 = r0 * (3072 - 192) + cg * 192 + id0 * 16;
        c.vh1 = r1 * (3072 - 192) + cg * 192 + id1 * 16;
        c.vh2 = r2 * (3072 - 192) + cg * 192 + id2 * 16;
        const int cc = lane & 31, h = lane >> 5;
        c.bx = (wid & 3) * 4096 + cc * 128 + ((h ^ ((cc >> 1) & 7)) << 4);
        c.bh = (8 * h + q4) * 192 + (g & 1) * 32 + 8 * p4;
        if (cg < 15) {
            const int row = 8 * wid + (lane >> 4);
            c.vq0 = row * 3840 + cg * 256 + (((lane & 15) ^ ((row & 3) << 2)) << 4);
            c.bw = 16384 + (8 * h + q4) * 256 + (((2 * (wid >> 2)) ^ q4) << 6) + (g & 1) * 32 + 8 * p4;
            const int blk = (rb * 15 + cg) * 2 + (wid >> 2), rks = blk / 10, rcl = blk - rks * 10;
            c.vrt = (lane >> 3) * 1280 + (lane & 7) * 16; c.srt = (rks * 32 + (wid & 3) * 8) * 1280 + rcl * 128;
            c.brt = L_RT + (4 * g + q4) * 128 + (wid & 3) * 32 + 8 * p4;
            c.vq1 = 0; c.rd0 = rt_doff(rks, 4 * rcl + (wid & 3)); c.rd1 = rt_doff(rks, 4 * rcl + 4 + (wid & 3));
            if (cg < 10) { if (wid < 4) phase12<0, 0>(c, ws); else phase12<0, 1>(c, ws); }
            else { if (wid < 4) phase12<1, 0>(c, ws); else phase12<1, 1>(c, ws); }
        } else {
            const int hf = wid & 1, rho = lane >> 3;
            c.vq0 = (40 * rb + 8 * hf + rho) * 1280 + (lane & 7) * 16;
            c.vq1 = (40 * (rho >> 2) + 20 * hf + 16 + (rho & 3)) * 1280 + (lane & 7) * 16;
            const int id = rb * 3 + (wid >> 1);
            c.srt = (wid >> 1) < 3 && id < 40 ? (id & 3) * 160 * 1280 + (id >> 2) * 128 : OOB;
            c.bw = 16384 + (wid >> 1) * 4096 + (4 * g + q4) * 128 + (wid & 1) * 64 + 8 * p4;
            c.vrt = 0; c.brt = L_RT; c.rd0 = OOB; c.rd1 = OOB;
            dma16(make_srd(mkptr(ka[10], ka[11]), 1280), lane * 16, wid * 1024, L_GW + wid * 1024);
            if (wid < 4) phase12<2, 0>(c, ws); else phase12<2, 1>(c, ws);
        }
    }
    const rsrc_t rws = c.rws; const u32x4 sws = c.sws;
    const unsigned nonce = __builtin_amdgcn_readfirstlane(c.nonce);
    const int hp = 2 * xcd + (idx & 1), rb = idx >> 1;
    const int rt = (xcd >> 1) * 8 + (idx & 7), ct = (xcd & 1) * 4 + (idx >> 3);
    const int sb = (5 * hp) >> 1, odd = hp & 1;
    if (wid >= 4) {
        const int wq = wid - 4;
        const int qc = lane & 31, h = lane >> 5;
        unsigned dm[8];
#pragma unroll
        for (int i = 0; i < 8; ++i) { const int kk = 8 * (i >> 1) + 4 * h + 2 * (i & 1); dm[i] = (kk <= qc ? 0xffffu : 0u) | (kk + 1 <= qc ? 0xffff0000u : 0u); }
        STAMP(4);
        bool hsig;
        {
            unsigned v;
            asm volatile("ds_read_b32 %0, %1 offset:8\n\ts_waitcnt lgkmcnt(0)" : "=v"(v) : "v"(L_CNT) : "memory");
            hsig = __builtin_amdgcn_readfirstlane(v) != 4;
            if (hsig) signal(L_CNT, lane, nonce, rws, WS_FH + (c.rb * 16 + c.cg) * 4);
        }
        STAMP(9);
        asm volatile("s_barrier" ::: "memory");
        __builtin_amdgcn_s_setprio(1);
        STAMP(6);
        const float gate = *(LAS float*)(uintptr_t)(L_GT + wq * 128 + qc * 4);
        const int tfirst = 8 - 4 * rb - wq;
        unsigned orem[2];
        const f32x16 zero16 = {0.f, 0.f, 0.f, 0.f, 0.f, 0.f, 0.f, 0.f, 0.f, 0.f, 0.f, 0.f, 0.f, 0.f, 0.f, 0.f};
        const float NEG = -1e30f, SC = 0.22360679774997896f * 1.4426950408889634f;
        const int tt = 8 * rb + 2 * wq;
        const unsigned obase = WS_ACT + ((tt >> 2) * 68 * 4 + (tt & 3)) * 1024;
#pragma unroll 1
        for (int hh = 0; hh < 2; ++hh) {
            const int fb = odd * 8 + 4 * hh;
            const u32x4 qa = __builtin_bit_cast(u32x4, ldsr128(L_Q + hh * 4096 + wq * 1024 + lane * 16)), qb = __builtin_bit_cast(u32x4, ldsr128(L_Q + 4096 + hh * 4096 + wq * 1024 + lane * 16));
            const bool a0 = 4 * h >= fb, a1 = 8 + 4 * h >= fb, b0 = 4 * h < fb + 4, b1 = 8 + 4 * h < fb + 4;
            const unsigned kp = L_K + hh * 12288 + wq * 1024 + lane * 16;
            const int f = qc < 20 ? 16 * hh + fb + qc : (odd ? 0 : 47);
            const unsigned vp = L_V + wq * 1024 + (f >> 4) * 12288 + h * 256 + (f & 15) * 16;
            f32x16 sc[9];
            bf16x8 kf[9][2];
            constexpr int ord[9] = {8, 0, 1, 2, 3, 4, 5, 6, 7};
#pragma unroll
            for (int i = 0; i < 3; ++i) { kf[i][0] = ldsr128(kp + ord[i] * 1024); kf[i][1] = ldsr128(kp + 12288 + ord[i] * 1024); }
            const bf16x8 qf0 = __builtin_bit_cast(bf16x8, (u32x4){a0 ? qa[0] : 0u, a0 ? qa[1] : 0u, a1 ? qa[2] : 0u, a1 ? qa[3] : 0u});
            const bf16x8 qf1 = __builtin_bit_cast(bf16x8, (u32x4){b0 ? qb[0] : 0u, b0 ? qb[1] : 0u, b1 ? qb[2] : 0u, b1 ? qb[3] : 0u});
            float mx[2] = {NEG, NEG};
#pragma unroll
            for (int i = 0; i < 11; ++i) {
                if (i < 9) { const int T = ord[i]; sc[T] = MFMA32(kf[i][0], qf0, zero16); sc[T] = MFMA32(kf[i][1], qf1, sc[T]); }
                if (i + 3 < 9) { kf[i + 3][0] = ldsr128(kp + ord[i + 3] * 1024); kf[i + 3][1] = ldsr128(kp + 12288 + ord[i + 3] * 1024); }
                if (i >= 3) {
                    const int T = ord[i - 2];
                    if (tfirst > T) {
                        SBAR();
#pragma unroll
                        for (int e = 0; e < 16; ++e) sc[T][e] = NEG;
                    }
                    if (T == 0) {
#pragma unroll
                        for (int e = 0; e < 16; ++e) sc[0][e] = 8 * (e >> 2) + 4 * h + (e & 3) <= qc ? sc[8][e] : sc[0][e];
                    }
#pragma unroll
                    for (int e = 0; e < 16; e += 4) { mx[0] = max3f(mx[0], sc[T][e], sc[T][e + 1]); mx[1] = max3f(mx[1], sc[T][e + 2], sc[T][e + 3]); }
                }
                SBAR();
            }
            float m = fmaxf(mx[0], mx[1]);
            { auto sw = __builtin_amdgcn_permlane32_swap(__float_as_uint(m), __float_as_uint(m), false, false); m = fmaxf(__uint_as_float(sw[0]), __uint_as_float(sw[1])); }
            const float nm = -m * SC;
            f32x16 o = zero16;
            bf16x8 vf[4];
            if (hh == 0) {
                STAMP(5);
                if (!hsig) signal(L_CNT, lane, nonce, rws, WS_FH + (c.rb * 16 + c.cg) * 4);
                lds_wait(L_CNT + 12);
            }
            vf[0] = ldsr128(vp); vf[1] = ldsr128(vp + 512); vf[2] = ldsr128(vp + 8192); vf[3] = ldsr128(vp + 8192 + 512);
#pragma unroll
            for (int T = 0; T < 8; ++T) {
                unsigned p[8];
#pragma unroll
                for (int e = 0; e < 8; ++e) {
                    float d0 = __builtin_fmaf(sc[T][2 * e], SC, nm), d1 = __builtin_fmaf(sc[T][2 * e + 1], SC, nm);
                    asm volatile("" : "+v"(d0));
                    p[e] = pk2(__builtin_amdgcn_exp2f(d0), __builtin_amdgcn_exp2f(d1));
                }
                if (T == 0) {
                    unsigned pd[8];
#pragma unroll
                    for (int e = 0; e < 8; ++e) { pd[e] = p[e] & dm[e]; p[e] ^= pd[e]; }
                    o = MFMA32(vf[2], __builtin_bit_cast(bf16x8, (u32x4){pd[0], pd[1], pd[2], pd[3]}), o);
                    o = MFMA32(vf[3], __builtin_bit_cast(bf16x8, (u32x4){pd[4], pd[5], pd[6], pd[7]}), o);
                }
                const int cur = (T & 1) * 2, nxt = 2 - cur;
                if (T < 7) { vf[nxt] = ldsr128(vp + (T + 1) * 1024); vf[nxt + 1] = ldsr128(vp + (T + 1) * 1024 + 512); }
                o = MFMA32(vf[cur], __builtin_bit_cast(bf16x8, (u32x4){p[0], p[1], p[2], p[3]}), o);
                o = MFMA32(vf[cur + 1], __builtin_bit_cast(bf16x8, (u32x4){p[4], p[5], p[6], p[7]}), o);
                SBAR();
            }
            const float cf = gate * __builtin_amdgcn_rcpf(o[12]);
            o *= cf;
            st128(pk8(o, 0, 4), sws, (qc >> 4) * 1024 + h * 256 + (qc & 15) * 16, obase + (48 + hp) * 4096 + hh * 512);
            const unsigned r0 = pk2(o[8], o[9]), r1 = pk2(o[10], o[11]);
            if (hh == 0) { orem[0] = r0; orem[1] = r1; }
            else st128((u32x4){orem[0], orem[1], r0, r1}, sws, h == 0 ? (qc >> 4) * 1024 + (qc & 15) * 16 : OOB, obase + (64 + (hp >> 2)) * 4096 + (hp & 3) * 256);
        }
        STAMP(7);
        signal(L_CNT + 4, lane, nonce, rws, WS_FO + (rb * 16 + hp) * 4);
        STAMP(8);
        STAMP_FLUSH();
        return;
    }
    const int mw = wid;
    const unsigned abase = WS_ACT + (rt * 68 + mw) * 4096, wbase = WS_WT + (ct * 68 + mw) * 5120;
    unsigned vo = lane * 16;
    OPAQUE(vo);
    bf16x8 W[17][5], A[17][4];
#define LDW(i) _Pragma("unroll") for (int n = 0; n < 5; ++n) W[i][n] = ld128(rws, vo + (n & 3) * 1024, wbase + (i) * 20480 + (n >> 2) * 4096)
#define LDA(i) _Pragma("unroll") for (int t = 0; t < 4; ++t) A[i][t] = ld128(rws, vo + t * 1024, abase + (i) * 16384)
    STAMP(3);
    {
        unsigned vh[2][3];
#pragma unroll
        for (int i = 0; i < 3; ++i) { const int id = wid * 64 + lane + 512 * i + 256, rr = id / 12; vh[1][i] = rr * (3072 - 192) + c.cg * 192 + id * 16; }
        vh[0][0] = c.vh0; vh[0][1] = c.vh1; vh[0][2] = c.vh2;
#pragma unroll
        for (int t = 12; t < 15; ++t)
#pragma unroll
            for (int j = 0; j < 2; ++j) dma16(c.sh, vh[j][t % 3], (t / 3) * (128 * 3072), t * 8192 + wid * 1024 + j * 4096);
        const bf16x8 rt0 = cat(trread(c.brt), trread(c.brt + 2048)), rt1 = cat(trread(c.brt + 4096), trread(c.brt + 6144));
        if (lane < 8) *(LAS unsigned*)(uintptr_t)(L_CNT + lane * 4) = 0u;
        wait_bar_lgkm<6>();
        st32(nonce, rws, wid == 0 && lane == 0 ? 0 : OOB, WS_FQ + (c.rb * 16 + c.cg) * 4);
        asm volatile("s_waitcnt vmcnt(1)" ::: "memory");
        lds_count(L_CNT + 16, lane);
        st128(__builtin_bit_cast(u32x4, rt0), sws, vo, c.rd0);
        st128(__builtin_bit_cast(u32x4, rt1), sws, vo, c.rd1);
    }
    STAMP(4);
    {
        const int kind = (lane >> 1) & 3, dr = (lane >> 3) & 3;
        int rr = rb - ((kind == 1 || kind == 2) && dr < 3 ? dr : 0); rr = rr < 0 ? 0 : rr;
        const int cc = kind == 3 ? 15 : 5 * kind + ((sb + 2 * (lane & 1)) >> 3);
        const unsigned fo = WS_FQ + (rr * 16 + cc) * 4;
        unsigned v;
        asm volatile("global_load_dword %0, %1, %2 sc1\n\ts_waitcnt vmcnt(0)" : "=v"(v) : "v"(fo), "s"(ws) : "memory");
        signal(L_CNT + 20, lane, nonce, rws, WS_FW + blockIdx.x * 4);
        if (!__all(v == nonce)) poll(ws, fo, nonce);
    }
    STAMP(5);
    {
        u32x4 sg[21];
        unsigned tpo[3];
#pragma unroll
        for (int j = 0; j < 3; ++j) { int gtp = 4 * rb - 8 + 4 * j + mw; gtp = gtp < 0 ? 0 : gtp; tpo[j] = WS_QKV + (sb * 64 + gtp) * 1024; }
#pragma unroll
        for (int i = 0; i < 9; ++i) sg[i] = __builtin_bit_cast(u32x4, ld128(rws, vo, tpo[i % 3] + (40 + i / 3) * 65536));
#pragma unroll
        for (int i = 0; i < 3; ++i) sg[9 + i] = __builtin_bit_cast(u32x4, ld128(rws, vo, tpo[2] + i * 65536));
        const unsigned gt = __builtin_amdgcn_raw_buffer_load_b32(rws, (lane & 31) * 4, WS_G + (rb * 128 + mw * 32) * 4, SC1);
#pragma unroll
        for (int i = 0; i < 9; ++i) sg[12 + i] = __builtin_bit_cast(u32x4, ld128(rws, vo, tpo[i % 3] + (80 + i / 3) * 65536));
        SBAR();
        const unsigned la = mw * 1024 + lane * 16;
#pragma unroll
        for (int i = 0; i < 9; ++i) *(LAS u32x4*)(uintptr_t)(la + L_K + i * 4096) = sg[i];
#pragma unroll
        for (int i = 0; i < 3; ++i) *(LAS u32x4*)(uintptr_t)(la + L_Q + i * 4096) = sg[9 + i];
        *(LAS unsigned*)(uintptr_t)(L_GT + mw * 128 + (lane & 31) * 4) = gt;
        lds_count(L_CNT + 8, lane);
        asm volatile("s_waitcnt lgkmcnt(0)\n\ts_barrier" ::: "memory");
        STAMP(6);
        const bool one0 = odd && r == 0, one2 = !odd && r == 15;
#pragma unroll
        for (int i = 0; i < 9; ++i) {
            u32x4 v = sg[12 + i];
            if (i < 3 || i >= 6) {
#pragma unroll
                for (int e = 0; e < 4; ++e) v[e] = (i < 3 ? one0 : one2) ? 0x3f803f80u : v[e];
            }
            *(LAS u32x4*)(uintptr_t)(la + L_V + i * 4096) = v;
        }
    }
    lds_count(L_CNT + 12, lane);
    poll4(ws, WS_FW + vo, nonce);
    STAMP(29);
    if (blockIdx.x == 0 && wid == 0) st32((nonce - 0x7F4A7C15u) * 0x0E8B2F51u + 1u, rws, lane == 0 ? 0 : OOB, WS_E);
#pragma unroll
    for (int i = 0; i < 5; ++i) { LDW(i); }
    SBAR();
    OPAQUE(vo);
    poll(ws, WS_FH + (rt >> 1) * 64 + ((vo >> 2) & 60), nonce);
    STAMP(7);
    f32x4 acc[4][5];
    const f32x4 zero4 = {0.f, 0.f, 0.f, 0.f};
#define MMA(i) _Pragma("unroll") for (int n = 0; n < 5; ++n) _Pragma("unroll") for (int t = 0; t < 4; ++t) acc[t][n] = MFMA(W[i][n], A[i][t], (i) == 0 ? zero4 : acc[t][n])
    LDA(0); LDA(1); LDA(2); LDA(3);
    float gate[4];
    SBAR();
#pragma unroll
    for (int i = 0; i < 12; ++i) {
#pragma unroll
        for (int n = 0; n < 5; ++n) {
#pragma unroll
            for (int t = 0; t < 4; ++t) acc[t][n] = MFMA(W[i][n], A[i][t], i == 0 ? zero4 : acc[t][n]);
            SBAR();
            const int j = 5 * i + n + 23, ji = j / 5, jn = j % 5;
            if (j >= 25) W[ji][jn] = ld128(rws, vo + (jn & 3) * 1024, wbase + ji * 20480 + (jn >> 2) * 4096);
            SBAR();
        }
        if (i + 4 < 12) { LDA(i + 4); }
        if (i == 8) {
            OPAQUE(vo);
#pragma unroll
            for (int t = 0; t < 4; ++t) gate[t] = __uint_as_float(__builtin_amdgcn_raw_buffer_load_b32(rws, (vo >> 2) & 60, WS_G + (rt * 64 + t * 16) * 4, SC1));
        }
        SBAR();
    }
    W[16][3] = ld128(rws, vo + 3072, wbase + 16 * 20480); W[16][4] = ld128(rws, vo, wbase + 16 * 20480 + 4096);
#pragma unroll
    for (int t = 0; t < 4; ++t) {
        const float og = bfround(1.0f - gate[t]);
#pragma unroll
        for (int n = 0; n < 5; ++n) { acc[t][n] *= og; OPAQUE(acc[t][n]); }
    }
    STAMP(8);
    OPAQUE(vo);
    poll(ws, WS_FO + (rt >> 1) * 64 + ((vo >> 2) & 60), nonce);
    STAMP(9);
    LDA(12); LDA(13); LDA(14);
    A[15][0] = ld128(rws, vo, abase + 15 * 16384); A[15][1] = ld128(rws, vo + 1024, abase + 15 * 16384); SBAR();
    MMA(12); SBAR();
    A[15][2] = ld128(rws, vo + 2048, abase + 15 * 16384); A[15][3] = ld128(rws, vo + 3072, abase + 15 * 16384);
    LDA(16); SBAR();
    MMA(13); SBAR(); MMA(14); SBAR(); MMA(15); SBAR(); MMA(16); SBAR();
    STAMP(10);
    OPAQUE(vo);
#pragma unroll
    for (int t = 0; t < 4; ++t) {
        if (t != mw) {
            const int slot = mw > t ? mw - 1 : mw;
#pragma unroll
            for (int n = 0; n < 5; ++n) *(LAS f32x4*)(uintptr_t)(L_RED + ((t * 3 + slot) * 5 + n) * 1024 + vo) = acc[t][n];
        }
    }
    asm volatile("s_waitcnt lgkmcnt(0)\n\ts_barrier" ::: "memory");
    f32x4 fin[5];
#pragma unroll
    for (int n = 0; n < 5; ++n) fin[n] = mw == 0 ? acc[0][n] : mw == 1 ? acc[1][n] : mw == 2 ? acc[2][n] : acc[3][n];
#pragma unroll
    for (int s = 0; s < 3; ++s)
#pragma unroll
        for (int n = 0; n < 5; ++n) fin[n] += *(LAS f32x4*)(uintptr_t)(L_RED + ((mw * 3 + s) * 5 + n) * 1024 + vo);
    u16* op = (u16*)mkptr(ka[14], ka[15]) + (size_t)(rt * 64 + mw * 16 + ((vo >> 4) & 15)) * DM + ct * 80 + (vo >> 8) * 4;
#pragma unroll
    for (int n = 0; n < 5; ++n) *(GAS u32x2*)(op + n * 16) = (u32x2){pk2(fin[n][0], fin[n][1]), pk2(fin[n][2], fin[n][3])};
    STAMP(11);
    STAMP_FLUSH();
}

extern "C" void kernel_launch(void* const* d_in, const int* in_sizes, int n_in, void* d_out, int out_size, void* d_ws, size_t ws_size, hipStream_t stream) {
    fused_kernel<<<256, 512, 0, stream>>>((const u16*)d_in[0], (const u16*)d_in[1], (const u16*)d_in[4], (const u16*)d_in[5], (const u16*)d_in[2], (const u16*)d_in[6], (char*)d_ws, (u16*)d_out);
}
```

```cpp
#define PADID 0
#define PADA 0
#include <hip/hip_runtime.h>
#include <stdint.h>

typedef unsigned short u16;
typedef __attribute__((ext_vector_type(8))) __bf16 bf16x8;
typedef __attribute__((ext_vector_type(2))) __bf16 bf16x2;
typedef __attribute__((ext_vector_type(4))) float f32x4;
typedef __attribute__((ext_vector_type(2))) float f32x2;
typedef __attribute__((ext_vector_type(16))) float f32x16;
typedef __attribute__((ext_vector_type(4))) unsigned u32x4;
typedef __attribute__((ext_vector_type(2))) unsigned u32x2;
typedef __attribute__((ext_vector_type(16))) unsigned u32x16;
#define LAS __attribute__((address_space(3)))
#define GAS __attribute__((address_space(1)))
#define DI __device__ __forceinline__
typedef LAS char* lptr;
typedef __amdgpu_buffer_rsrc_t rsrc_t;

constexpr int SEQ = 2048, DM = 640, NQKV = 1920, DFF = 1536;
constexpr unsigned WS_QKV = 0;
constexpr unsigned WS_ACT = WS_QKV + 120 * 64 * 1024;
constexpr unsigned WS_WT = WS_ACT + 32 * 68 * 4096;
constexpr unsigned WS_G = WS_WT + 8 * 68 * 5120;
constexpr unsigned WS_FQ = WS_G + 8192;
constexpr unsigned WS_FH = WS_FQ + 1024;
constexpr unsigned WS_FO = WS_FH + 1024;
constexpr unsigned WS_FW = WS_FO + 1024;
constexpr unsigned WS_E = WS_FW + 1024;
constexpr unsigned WS_END = WS_E + 256;
constexpr unsigned WS_STAMP = WS_END;
constexpr unsigned OOB = 0x7f000000u;
constexpr int SC1 = 16;

#define MFMA(a, b, c) __builtin_amdgcn_mfma_f32_16x16x32_bf16((a), (b), (c), 0, 0, 0)
#define MFMA32(a, b, c) __builtin_amdgcn_mfma_f32_32x32x16_bf16((a), (b), (c), 0, 0, 0)
#define SBAR() __builtin_amdgcn_sched_barrier(0)
#define OPAQUE(v) asm volatile("" : "+v"(v))

DI unsigned pk2(float a, float b) { f32x2 v = {a, b}; return __builtin_bit_cast(unsigned, __builtin_convertvector(v, bf16x2)); }
DI float bfround(float a) { return __uint_as_float(pk2(a, 0.f) << 16); }
DI u32x4 make_srd(const void* p, unsigned bytes) {
    uint64_t a = (uint64_t)p; u32x4 r = {(unsigned)a, (unsigned)(a >> 32) & 0xffffu, bytes, 0x00020000u}; return r; }
DI void dma16(u32x4 srd, unsigned voff, unsigned soff, unsigned ldsaddr) {
    asm volatile("s_mov_b32 m0, %0\n\ts_nop 0\n\tbuffer_load_dwordx4 %1, %2, %3 offen lds" :: "s"(ldsaddr), "v"(voff), "s"(srd), "s"(soff) : "memory");
}
typedef short v4i16_t __attribute__((ext_vector_type(4)));
DI u32x2 trread(unsigned a) { return __builtin_bit_cast(u32x2, __builtin_amdgcn_ds_read_tr16_b64_v4i16((LAS v4i16_t*)(uintptr_t)a)); }
DI bf16x8 cat(u32x2 a, u32x2 b) { u32x4 v = {a[0], a[1], b[0], b[1]}; return __builtin_bit_cast(bf16x8, v); }
DI bf16x8 ldsr128(unsigned a) { return *(LAS bf16x8*)(uintptr_t)a; }
DI u32x2 ldsr64(unsigned a) { return *(LAS u32x2*)(uintptr_t)a; }
template <int N> DI void wait_bar() { asm volatile("s_waitcnt vmcnt(%0)\n\ts_barrier" :: "n"(N) : "memory"); }
template <int N> DI void wait_bar_lgkm() { asm volatile("s_waitcnt vmcnt(%0) lgkmcnt(0)\n\ts_barrier" :: "n"(N) : "memory"); }
DI void st128(u32x4 v, u32x4 srd, unsigned voff, unsigned soff) { asm volatile("buffer_store_dwordx4 %0, %1, %2, %3 offen sc1\n\ts_nop 1" :: "v"(v), "v"(voff), "s"(srd), "s"(soff) : "memory"); }
DI void st32(unsigned v, rsrc_t rs, unsigned voff, unsigned soff) { __builtin_amdgcn_raw_buffer_store_b32(v, rs, voff, soff, SC1); }
DI bf16x8 ld128(rsrc_t rs, unsigned voff, unsigned soff) { return __builtin_bit_cast(bf16x8, __builtin_amdgcn_raw_buffer_load_b128(rs, voff, soff, SC1)); }
DI void poll(const char* ws, unsigned off, unsigned nonce) {
#pragma unroll 1
    for (int spin = 0; spin < (1 << 18); ++spin) {
        unsigned v;
        asm volatile("global_load_dword %0, %1, %2 sc1\n\ts_waitcnt vmcnt(0)" : "=v"(v) : "v"(off), "s"(ws) : "memory");
        if (__all(v == nonce)) break;
        __builtin_amdgcn_s_sleep(1);
    }
}
DI void poll4(const char* ws, unsigned off, unsigned nonce) {
#pragma unroll 1
    for (int spin = 0; spin < (1 << 18); ++spin) {
        u32x4 v;
        asm volatile("global_load_dwordx4 %0, %1, %2 sc1\n\ts_waitcnt vmcnt(0)" : "=v"(v) : "v"(off), "s"(ws) : "memory");
        if (__all(v[0] == nonce && v[1] == nonce && v[2] == nonce && v[3] == nonce)) break;
        __builtin_amdgcn_s_sleep(1);
    }
}

#define STAMP(idx) do { if ((idx) == PADID) { SBAR(); c.t1 = __builtin_amdgcn_s_memrealtime(); SBAR(); } } while (0)
#define CSTAMP(idx) do {} while (0)
#define STAMP_FLUSH() do { if ((wid >= 4) == PADA) { while (__builtin_amdgcn_s_memrealtime() < c.t1 + 6000) __builtin_amdgcn_s_sleep(2); } } while (0)

constexpr int SLOT = 32768, LDS_BYTES = 5 * SLOT;
constexpr int sbase(int n) { return (n % 5) * SLOT; }
constexpr int L_RT = 122880;
constexpr int L_GW = 4 * SLOT + 24576;
constexpr int L_CNT = 0, L_Q = 1024, L_GT = 13312, L_V = 14336, L_RED = 53248, L_K = 126976;
#define SB12 56

struct Ctx {
    u32x4 sx, sq, sh, s2;
    rsrc_t rws; u32x4 sws;
    unsigned vx0, vx1, vq0, vq1, vh0, vh1, vh2, vrt, srt;
    unsigned bx, bw, bh, brt;
    unsigned lane16;
    int wid, rb, cg;
    unsigned nonce;
    unsigned rd0, rd1;
    unsigned long long t1;
};
constexpr int nWait(int role, int ch, int m) {
    if (role != 2) return m == 10 ? 9 : 8;
    return m == 0 ? 8 : m <= 4 ? 10 : m == 5 ? 8 : m == 6 ? 6 : m == 7 ? 4 : m == 8 ? 6 : 8;
}
template <int ROLE, int CH> DI void bar_stage(int m) {
    if (m == 1) wait_bar<nWait(ROLE, CH, 1)>(); if (m == 2) wait_bar<nWait(ROLE, CH, 2)>(); if (m == 3) wait_bar<nWait(ROLE, CH, 3)>(); if (m == 4) wait_bar<nWait(ROLE, CH, 4)>();
    if (m == 5) wait_bar<nWait(ROLE, CH, 5)>(); if (m == 6) wait_bar<nWait(ROLE, CH, 6)>(); if (m == 7) wait_bar<nWait(ROLE, CH, 7)>(); if (m == 8) wait_bar<nWait(ROLE, CH, 8)>();
    if (m == 9) wait_bar_lgkm<nWait(ROLE, CH, 9)>(); if (m == 10) wait_bar<nWait(ROLE, CH, 10)>();
}
DI unsigned rt_doff(int rks, int n16) { const int ct = (n16 * 205) >> 10, nt = n16 - ct * 5; return WS_WT + ((ct * 68 + rks) * 5 + nt) * 1024; }

template <int ROLE> DI void issue_piece(const Ctx& c, int n, int i) {
    const unsigned b = sbase(n);
    if (n < 10) {
        if (i == 0) dma16(c.sx, c.vx0, n * 128, b + c.wid * 2048);
        if (i == 1) dma16(c.sx, c.vx1, n * 128, b + c.wid * 2048 + 1024);
        if (ROLE != 2) {
            if (i == 2) dma16(c.sq, c.vq0, n * (64 * 3840), b + 16384 + c.wid * 2048);
            if (i == 3) dma16(c.sq, c.vq0, n * (64 * 3840) + 4 * 3840, b + 16384 + c.wid * 2048 + 1024);
        } else if (n < 3) {
            const int l = n * 4 + (c.wid >> 1);
            const unsigned so = l < 10 ? l * 128 : OOB;
            if (i == 2) dma16(c.sq, c.vq0, so, b + 16384 + c.wid * 2048);
            if (i == 3) dma16(c.sq, c.vq0, so + 20 * 1280, b + 16384 + c.wid * 2048 + 1024);
        } else if (n == 3) {
            if (i == 2) dma16(c.sq, c.vq1, c.srt, b + 16384 + c.wid * 2048);
            if (i == 3) dma16(c.sq, c.vq1, c.srt + 80 * 1280, b + 16384 + c.wid * 2048 + 1024);
        }
    } else {
        const int t = (n - 10) * 4 + i;
        if (n == 12 && i == 0 && ROLE != 2) dma16(c.s2, c.vrt, c.srt, L_RT + c.wid * 1024);
        if (t < 12) dma16(c.sh, t % 3 == 0 ? c.vh0 : t % 3 == 1 ? c.vh1 : c.vh2, (t / 3) * (128 * 3072), t * 8192 + c.wid * 1024);
    }
}
DI bf16x8 rd_wq(unsigned bw, int J, int u) {
    const int k = 2 * J + (u >> 1);
    const unsigned a = bw ^ (unsigned)(((u & 1) << 6) | sbase(k >> 2));
    return cat(trread(a + (k & 3) * 4096), trread(a + (k & 3) * 4096 + 1024));
}
constexpr int KT = 28;
constexpr int p2k(int s) { return s < 3 * KT ? s / 3 : KT + (s - 3 * KT) % (40 - KT); }
constexpr int p2v(int s) { return s < 3 * KT ? s % 3 : (s - 3 * KT) / (40 - KT); }
DI bf16x8 rd_wh(unsigned bh, int s) {
    const int k = p2k(s), v = p2v(s);
    const unsigned a = bh + (k >= 20 ? 61440 : 0);
    const int o = (k % 20) * 3072 + v * 64;
    return cat(trread(a + o), trread(a + o + 768));
}
DI bf16x8 rd_x(unsigned bx, int k) { return ldsr128(bx ^ (unsigned)(((k & 3) << 5) | sbase(k >> 2))); }
DI void retile1(const Ctx& c, unsigned a, unsigned doff) {
    const u32x2 a0 = trread(a), b0 = trread(a + 2048);
    st128((u32x4){a0[0], a0[1], b0[0], b0[1]}, c.sws, c.lane16, doff);
}
DI u32x4 pk8(const f32x16& a, int i, int j) { return (u32x4){pk2(a[i], a[i + 1]), pk2(a[i + 2], a[i + 3]), pk2(a[j], a[j + 1]), pk2(a[j + 2], a[j + 3])}; }

DI void signal(unsigned cnt, int lane, unsigned nonce, rsrc_t rws, unsigned flag) {
    asm volatile("s_waitcnt vmcnt(0)" ::: "memory");
    if (lane == 0) {
        unsigned old;
        asm volatile("ds_add_rtn_u32 %0, %1, %2\n\ts_waitcnt lgkmcnt(0)" : "=v"(old) : "v"(cnt), "v"(1) : "memory");
        if (old == 3) st32(nonce, rws, 0, flag);
    }
}
DI void lds_count(unsigned cnt, int lane) { if (lane == 0) asm volatile("s_waitcnt lgkmcnt(0)\n\tds_add_u32 %0, %1" :: "v"(cnt), "v"(1) : "memory"); }
DI void lds_wait(unsigned cnt) {
#pragma unroll 1
    for (int spin = 0; spin < (1 << 20); ++spin) {
        unsigned v;
        asm volatile("ds_read_b32 %0, %1\n\ts_waitcnt lgkmcnt(0)" : "=v"(v) : "v"(cnt) : "memory");
        if (__builtin_amdgcn_readfirstlane(v) == 4) break;
    }
}
template <int I> struct IC { static constexpr int v = I; };
template <int A, int B, class F> DI void sfor(F&& f) { if constexpr (A < B) { f(IC<A>{}); sfor<A + 1, B>(f); } }

template <int ROLE, int CH>
DI void phase12(Ctx& c, char* ws) {
    constexpr int NW = 5;
    bf16x8 xf[40], wf[NW];
    f32x16 acc[2];
    const f32x16 zero16 = {0.f, 0.f, 0.f, 0.f, 0.f, 0.f, 0.f, 0.f, 0.f, 0.f, 0.f, 0.f, 0.f, 0.f, 0.f, 0.f};
    const int tq = c.wid & 3;
    const int lane = c.lane16 >> 4;
    issue_piece<ROLE>(c, 0, 2); issue_piece<ROLE>(c, 0, 3);
#pragma unroll
    for (int n = 1; n < 3; ++n)
#pragma unroll
        for (int i = 0; i < 4; ++i) issue_piece<ROLE>(c, n, i);
    unsigned e;
    asm volatile("s_load_dword %0, %1, %3\n\ts_waitcnt vmcnt(%2) lgkmcnt(0)\n\ts_barrier" : "=s"(e) : "s"(ws), "n"(nWait(ROLE, CH, 0)), "s"(WS_E) : "memory");
    c.nonce = e * 0x9E3779B1u + 0x7F4A7C15u;
    STAMP(1); CSTAMP(12);
    if (ROLE != 2) {
        xf[0] = rd_x(c.bx, 0); xf[1] = rd_x(c.bx, 1);
#pragma unroll
        for (int u = 0; u < 4; ++u) wf[u] = rd_wq(c.bw, 0, u);
        SBAR();
#pragma unroll
        for (int J = 0; J < 20; ++J) {
            OPAQUE(c.bx); OPAQUE(c.bw);
#pragma unroll
            for (int u = 0; u < 4; ++u) {
                const int k = 2 * J + (u >> 1), v = u & 1;
                const f32x16 ci = k == 0 ? zero16 : acc[v];
                acc[v] = ROLE == 1 ? MFMA32(xf[k], wf[u], ci) : MFMA32(wf[u], xf[k], ci);
                SBAR();
                if (u == 0 && (J & 1)) {
                    bar_stage<ROLE, CH>((J + 1) >> 1); STAMP(16 + ((J + 1) >> 1));
                }
                if (u < 2 && J < 19) xf[2 * J + 2 + u] = rd_x(c.bx, 2 * J + 2 + u);
                if (J < 19) wf[u] = rd_wq(c.bw, J + 1, u);
                else if (CH) { wf[u] = rd_wh(c.bh, u); if (u == 3) wf[4] = rd_wh(c.bh, 4); }
                if (J == 0) issue_piece<ROLE>(c, 3, u);
                else if (u >= 2) issue_piece<ROLE>(c, ((J + 1) >> 1) + 3, (u - 2) + ((J & 1) ? 0 : 2));
                SBAR();
            }
        }
    } else {
        f32x4 z0 = {0.f, 0.f, 0.f, 0.f}, z1 = z0;
        const unsigned ga = L_GW + (((lane ^ (lane >> 4)) & 1) ? 2048 : (lane >> 5) * 16);
#pragma unroll
        for (int m = 0; m <= 10; ++m) {
            bar_stage<2, CH>(m);
#pragma unroll
            for (int i = 0; i < 4; ++i) issue_piece<2>(c, m + 3, i);
            if (m < 10) {
                OPAQUE(c.bx);
#pragma unroll
                for (int i = 0; i < 4; ++i) xf[4 * m + i] = rd_x(c.bx, 4 * m + i);
                if (m < 4) {
                    const int l = m * 4 + (c.wid >> 1), id = c.rb * 3 + (c.wid >> 1);
                    const bool valid = m < 3 ? l < 10 : (c.wid >> 1) < 3 && id < 40;
                    const int rks = m < 3 ? 48 + c.rb : 64 + (id & 3), n16 = 4 * (m < 3 ? l : id >> 2) + (c.wid & 1) * 2;
                    retile1(c, c.bw + sbase(m), valid ? rt_doff(rks, n16) : OOB);
                    retile1(c, c.bw + sbase(m) + 32, valid ? rt_doff(rks, n16 + 1) : OOB);
                }
                if (CH == 0) {
#pragma unroll
                    for (int i = 0; i < 4; i += 2) { z0 = MFMA(ldsr128(ga + (4 * m + i) * 32), xf[4 * m + i], z0); z1 = MFMA(ldsr128(ga + (4 * m + i + 1) * 32), xf[4 * m + i + 1], z1); }
                } else asm volatile("s_waitcnt lgkmcnt(0)" ::: "memory");
            }
            SBAR();
        }
        if (CH) {
#pragma unroll
            for (int u = 0; u < NW; ++u) wf[u] = rd_wh(c.bh, u);
        } else {
            z0 += z1;
            const float z = bfround((lane & 16) ? z0[1] : z0[0]);
            const float gt = bfround(1.0f / (1.0f + __expf(-z)));
            st32(__float_as_uint(gt), c.rws, lane < 32 ? lane * 4 : OOB, WS_G + (c.rb * 128 + tq * 32) * 4);
        }
    }
    STAMP(2); CSTAMP(13);
    const unsigned so = WS_QKV + ((8 * c.cg + 4 * CH) * 64 + 4 * c.rb + tq) * 1024;
    const unsigned vst = ROLE == 0 ? c.lane16 : ((lane >> 4) & 1) * 65536 + (lane >> 5) * 256 + (lane & 15) * 16;
    constexpr unsigned sd = ROLE == 0 ? 65536 : 512;
    if (CH == 0) {
        if (ROLE != 2) {
#pragma unroll
            for (int v = 0; v < 2; ++v) { st128(pk8(acc[v], 0, 4), c.sws, vst, so + v * 131072); st128(pk8(acc[v], 8, 12), c.sws, vst, so + v * 131072 + sd); }
        }
        wait_bar<ROLE != 2 ? 8 : 5>();
        return;
    }
    f32x16 hc[3];
    u32x4 pq[4];
    unsigned cw;
    const int tt = 8 * c.rb + 2 * tq;
    const unsigned sa = WS_ACT + (((tt >> 2) * 68 + 3 * c.cg) * 4 + (tt & 3)) * 1024;
    const unsigned vo = ((lane >> 4) & 1) * 1024 + (lane >> 5) * 256 + (lane & 15) * 16;
    auto hstore = [&](int v, int half) __attribute__((always_inline)) {
        f32x16 a = hc[v];
#pragma unroll
        for (int i = 0; i < 4; ++i) { a[half * 4 + i] = fmaxf(a[half * 4 + i], 0.f); a[half * 4 + 8 + i] = fmaxf(a[half * 4 + 8 + i], 0.f); }
        st128(pk8(a, half * 4, half * 4 + 8), c.sws, vo, sa + v * 4096 + half * 512);
    };
    constexpr int SW = 3 * KT + (32 - KT) - NW;
    sfor<0, 120>([&](auto S) __attribute__((always_inline)) {
        constexpr int s = S.v, k = p2k(s), v = p2v(s);
        hc[v] = MFMA32(wf[s % NW], xf[k], k == 0 ? zero16 : hc[v]);
        SBAR();
        if (s == 10) { wait_bar<ROLE != 2 ? 8 : 4>(); STAMP(27); }
        if (s == SB12) { CSTAMP(14); wait_bar<0>(); CSTAMP(15); STAMP(28); }
        if (s == SW - 4) cw = *(volatile LAS unsigned*)(uintptr_t)(L_CNT + 16);
        if (s == SW) { if (__builtin_amdgcn_readfirstlane(cw) != 4) lds_wait(L_CNT + 16); }
        if (s + NW < 120) wf[s % NW] = rd_wh(c.bh, s + NW);
        if constexpr (s >= 3 * KT + (40 - KT) && (k == KT + 2 || k == KT + 5)) hstore(v - 1, k == KT + 5);
        if (ROLE != 2) {
            if constexpr (s < 2) { pq[2 * s] = pk8(acc[s], 0, 4); pq[2 * s + 1] = pk8(acc[s], 8, 12); }
            if constexpr (s >= 30 && s < 34) { OPAQUE(c.bx); xf[s + 6] = rd_x(c.bx, s + 6); }
            if constexpr (s < 8 && (s & 1)) { constexpr int q = s >> 1; st128(pq[q], c.sws, vst, so + (q >> 1) * 131072 + (q & 1) * sd); }
        }
        SBAR();
    });
    STAMP(3);
    hstore(2, 0); hstore(2, 1);
}

DI float max3f(float a, float b, float c) { return fmaxf(fmaxf(a, b), c); }
DI const void* mkptr(unsigned lo, unsigned hi) { return (const void*)(((uint64_t)hi << 32) | lo); }

__global__ __launch_bounds__(512, 2) void fused_kernel(const u16* x_, const u16* wqkv_, const u16* w1_, const u16* w2_, const u16* wo_, const u16* gw_, char* ws_, u16* out_) {
    __shared__ __attribute__((aligned(16))) char smem[LDS_BYTES];
    Ctx c;
    u32x16 ka;
    asm volatile("s_load_dwordx16 %0, %1, 0x0" : "=s"(ka) : "s"(__builtin_amdgcn_kernarg_segment_ptr()) : "memory");
    STAMP(0);
    const int lane = threadIdx.x & 63;
    const int wid = __builtin_amdgcn_readfirstlane(threadIdx.x >> 6);
    const int r = lane & 15, g = lane >> 4, q4 = r >> 2, p4 = r & 3;
    const int xcd = blockIdx.x & 7, idx = blockIdx.x >> 3;
    char* ws;
    {
        const int rb = (xcd & 1) * 8 + (idx & 7), cg = (xcd >> 1) * 4 + (idx >> 3);
        c.wid = wid; c.rb = rb; c.cg = cg; c.lane16 = lane * 16;
        const int t0 = 16 * wid + (lane >> 3), t1 = t0 + 8;
        c.vx0 = (rb * 128 + t0) * 1280 + (((lane & 7) ^ ((t0 >> 1) & 7)) << 4);
        c.vx1 = (rb * 128 + t1) * 1280 + (((lane & 7) ^ ((t1 >> 1) & 7)) << 4);
        asm volatile("s_waitcnt lgkmcnt(0)" : "+s"(ka), "+v"(c.vx0), "+v"(c.vx1) :: "memory");
        asm volatile("" :: "v"((LAS char*)smem) : "memory");
        c.sx = make_srd(mkptr(ka[0], ka[1]), SEQ * DM * 2);
        issue_piece<0>(c, 0, 0); issue_piece<0>(c, 0, 1);
        SBAR();
        ws = (char*)mkptr(ka[12], ka[13]);
        c.sq = cg < 15 ? make_srd(mkptr(ka[2], ka[3]), DM * NQKV * 2) : make_srd(mkptr(ka[8], ka[9]), DM * DM * 2);
        c.sh = make_srd(mkptr(ka[4], ka[5]), DM * DFF * 2); c.s2 = make_srd(mkptr(ka[6], ka[7]), DFF * DM * 2);
        c.sws = make_srd(ws, WS_END);
        c.rws = __builtin_amdgcn_make_buffer_rsrc(ws, 0, WS_END, 0x00020000);
        const int id0 = wid * 64 + lane, id1 = id0 + 512, id2 = id0 + 1024;
        const int r0 = id0 / 12, r1 = id1 / 12, r2 = id2 / 12;
        c.vh0 = r0 * (3072 - 192) + cg * 192 + id0 * 16;
        c.vh1 = r1 * (3072 - 192) + cg * 192 + id1 * 16;
        c.vh2 = r2 * (3072 - 192) + cg * 192 + id2 * 16;
        const int cc = lane & 31, h = lane >> 5;
        c.bx = (wid & 3) * 4096 + cc * 128 + ((h ^ ((cc >> 1) & 7)) << 4);
        c.bh = (8 * h + q4) * 192 + (g & 1) * 32 + 8 * p4;
        if (cg < 15) {
            const int row = 8 * wid + (lane >> 4);
            c.vq0 = row * 3840 + cg * 256 + (((lane & 15) ^ ((row & 3) << 2)) << 4);
            c.bw = 16384 + (8 * h + q4) * 256 + (((2 * (wid >> 2)) ^ q4) << 6) + (g & 1) * 32 + 8 * p4;
            const int blk = (rb * 15 + cg) * 2 + (wid >> 2), rks = blk / 10, rcl = blk - rks * 10;
            c.vrt = (lane >> 3) * 1280 + (lane & 7) * 16; c.srt = (rks * 32 + (wid & 3) * 8) * 1280 + rcl * 128;
            c.brt = L_RT + (4 * g + q4) * 128 + (wid & 3) * 32 + 8 * p4;
            c.vq1 = 0; c.rd0 = rt_doff(rks, 4 * rcl + (wid & 3)); c.rd1 = rt_doff(rks, 4 * rcl + 4 + (wid & 3));
            if (cg < 10) { if (wid < 4) phase12<0, 0>(c, ws); else phase12<0, 1>(c, ws); }
            else { if (wid < 4) phase12<1, 0>(c, ws); else phase12<1, 1>(c, ws); }
        } else {
            const int hf = wid & 1, rho = lane >> 3;
            c.vq0 = (40 * rb + 8 * hf + rho) * 1280 + (lane & 7) * 16;
            c.vq1 = (40 * (rho >> 2) + 20 * hf + 16 + (rho & 3)) * 1280 + (lane & 7) * 16;
            const int id = rb * 3 + (wid >> 1);
            c.srt = (wid >> 1) < 3 && id < 40 ? (id & 3) * 160 * 1280 + (id >> 2) * 128 : OOB;
            c.bw = 16384 + (wid >> 1) * 4096 + (4 * g + q4) * 128 + (wid & 1) * 64 + 8 * p4;
            c.vrt = 0; c.brt = L_RT; c.rd0 = OOB; c.rd1 = OOB;
            dma16(make_srd(mkptr(ka[10], ka[11]), 1280), lane * 16, wid * 1024, L_GW + wid * 1024);
            if (wid < 4) phase12<2, 0>(c, ws); else phase12<2, 1>(c, ws);
        }
    }
    const rsrc_t rws = c.rws; const u32x4 sws = c.sws;
    const unsigned nonce = __builtin_amdgcn_readfirstlane(c.nonce);
    const int hp = 2 * xcd + (idx & 1), rb = idx >> 1;
    const int rt = (xcd >> 1) * 8 + (idx & 7), ct = (xcd & 1) * 4 + (idx >> 3);
    const int sb = (5 * hp) >> 1, odd = hp & 1;
    if (wid >= 4) {
        const int wq = wid - 4;
        const int qc = lane & 31, h = lane >> 5;
        unsigned dm[8];
#pragma unroll
        for (int i = 0; i < 8; ++i) { const int kk = 8 * (i >> 1) + 4 * h + 2 * (i & 1); dm[i] = (kk <= qc ? 0xffffu : 0u) | (kk + 1 <= qc ? 0xffff0000u : 0u); }
        STAMP(4);
        bool hsig;
        {
            unsigned v;
            asm volatile("ds_read_b32 %0, %1 offset:8\n\ts_waitcnt lgkmcnt(0)" : "=v"(v) : "v"(L_CNT) : "memory");
            hsig = __builtin_amdgcn_readfirstlane(v) != 4;
            if (hsig) signal(L_CNT, lane, nonce, rws, WS_FH + (c.rb * 16 + c.cg) * 4);
        }
        STAMP(9);
        asm volatile("s_barrier" ::: "memory");
        __builtin_amdgcn_s_setprio(1);
        STAMP(6);
        const float gate = *(LAS float*)(uintptr_t)(L_GT + wq * 128 + qc * 4);
        const int tfirst = 8 - 4 * rb - wq;
        unsigned orem[2];
        const f32x16 zero16 = {0.f, 0.f, 0.f, 0.f, 0.f, 0.f, 0.f, 0.f, 0.f, 0.f, 0.f, 0.f, 0.f, 0.f, 0.f, 0.f};
        const float NEG = -1e30f, SC = 0.22360679774997896f * 1.4426950408889634f;
        const int tt = 8 * rb + 2 * wq;
        const unsigned obase = WS_ACT + ((tt >> 2) * 68 * 4 + (tt & 3)) * 1024;
#pragma unroll 1
        for (int hh = 0; hh < 2; ++hh) {
            const int fb = odd * 8 + 4 * hh;
            const u32x4 qa = __builtin_bit_cast(u32x4, ldsr128(L_Q + hh * 4096 + wq * 1024 + lane * 16)), qb = __builtin_bit_cast(u32x4, ldsr128(L_Q + 4096 + hh * 4096 + wq * 1024 + lane * 16));
            const bool a0 = 4 * h >= fb, a1 = 8 + 4 * h >= fb, b0 = 4 * h < fb + 4, b1 = 8 + 4 * h < fb + 4;
            const unsigned kp = L_K + hh * 12288 + wq * 1024 + lane * 16;
            const int f = qc < 20 ? 16 * hh + fb + qc : (odd ? 0 : 47);
            const unsigned vp = L_V + wq * 1024 + (f >> 4) * 12288 + h * 256 + (f & 15) * 16;
            f32x16 sc[9];
            bf16x8 kf[9][2];
            constexpr int ord[9] = {8, 0, 1, 2, 3, 4, 5, 6, 7};
#pragma unroll
            for (int i = 0; i < 3; ++i) { kf[i][0] = ldsr128(kp + ord[i] * 1024); kf[i][1] = ldsr128(kp + 12288 + ord[i] * 1024); }
            const bf16x8 qf0 = __builtin_bit_cast(bf16x8, (u32x4){a0 ? qa[0] : 0u, a0 ? qa[1] : 0u, a1 ? qa[2] : 0u, a1 ? qa[3] : 0u});
            const bf16x8 qf1 = __builtin_bit_cast(bf16x8, (u32x4){b0 ? qb[0] : 0u, b0 ? qb[1] : 0u, b1 ? qb[2] : 0u, b1 ? qb[3] : 0u});
            float mx[2] = {NEG, NEG};
#pragma unroll
            for (int i = 0; i < 11; ++i) {
                if (i < 9) { const int T = ord[i]; sc[T] = MFMA32(kf[i][0], qf0, zero16); sc[T] = MFMA32(kf[i][1], qf1, sc[T]); }
                if (i + 3 < 9) { kf[i + 3][0] = ldsr128(kp + ord[i + 3] * 1024); kf[i + 3][1] = ldsr128(kp + 12288 + ord[i + 3] * 1024); }
                if (i >= 3) {
                    const int T = ord[i - 2];
                    if (tfirst > T) {
                        SBAR();
#pragma unroll
                        for (int e = 0; e < 16; ++e) sc[T][e] = NEG;
                    }
                    if (T == 0) {
#pragma unroll
                        for (int e = 0; e < 16; ++e) sc[0][e] = 8 * (e >> 2) + 4 * h + (e & 3) <= qc ? sc[8][e] : sc[0][e];
                    }
#pragma unroll
                    for (int e = 0; e < 16; e += 4) { mx[0] = max3f(mx[0], sc[T][e], sc[T][e + 1]); mx[1] = max3f(mx[1], sc[T][e + 2], sc[T][e + 3]); }
                }
                SBAR();
            }
            float m = fmaxf(mx[0], mx[1]);
            { auto sw = __builtin_amdgcn_permlane32_swap(__float_as_uint(m), __float_as_uint(m), false, false); m = fmaxf(__uint_as_float(sw[0]), __uint_as_float(sw[1])); }
            const float nm = -m * SC;
            f32x16 o = zero16;
            bf16x8 vf[4];
            if (hh == 0) {
                STAMP(5);
                if (!hsig) signal(L_CNT, lane, nonce, rws, WS_FH + (c.rb * 16 + c.cg) * 4);
                lds_wait(L_CNT + 12);
            }
            vf[0] = ldsr128(vp); vf[1] = ldsr128(vp + 512); vf[2] = ldsr128(vp + 8192); vf[3] = ldsr128(vp + 8192 + 512);
#pragma unroll
            for (int T = 0; T < 8; ++T) {
                unsigned p[8];
#pragma unroll
                for (int e = 0; e < 8; ++e) {
                    float d0 = __builtin_fmaf(sc[T][2 * e], SC, nm), d1 = __builtin_fmaf(sc[T][2 * e + 1], SC, nm);
                    asm volatile("" : "+v"(d0));
                    p[e] = pk2(__builtin_amdgcn_exp2f(d0), __builtin_amdgcn_exp2f(d1));
                }
                if (T == 0) {
                    unsigned pd[8];
#pragma unroll
                    for (int e = 0; e < 8; ++e) { pd[e] = p[e] & dm[e]; p[e] ^= pd[e]; }
                    o = MFMA32(vf[2], __builtin_bit_cast(bf16x8, (u32x4){pd[0], pd[1], pd[2], pd[3]}), o);
                    o = MFMA32(vf[3], __builtin_bit_cast(bf16x8, (u32x4){pd[4], pd[5], pd[6], pd[7]}), o);
                }
                const int cur = (T & 1) * 2, nxt = 2 - cur;
                if (T < 7) { vf[nxt] = ldsr128(vp + (T + 1) * 1024); vf[nxt + 1] = ldsr128(vp + (T + 1) * 1024 + 512); }
                o = MFMA32(vf[cur], __builtin_bit_cast(bf16x8, (u32x4){p[0], p[1], p[2], p[3]}), o);
                o = MFMA32(vf[cur + 1], __builtin_bit_cast(bf16x8, (u32x4){p[4], p[5], p[6], p[7]}), o);
                SBAR();
            }
            const float cf = gate * __builtin_amdgcn_rcpf(o[12]);
            o *= cf;
            st128(pk8(o, 0, 4), sws, (qc >> 4) * 1024 + h * 256 + (qc & 15) * 16, obase + (48 + hp) * 4096 + hh * 512);
            const unsigned r0 = pk2(o[8], o[9]), r1 = pk2(o[10], o[11]);
            if (hh == 0) { orem[0] = r0; orem[1] = r1; }
            else st128((u32x4){orem[0], orem[1], r0, r1}, sws, h == 0 ? (qc >> 4) * 1024 + (qc & 15) * 16 : OOB, obase + (64 + (hp >> 2)) * 4096 + (hp & 3) * 256);
        }
        STAMP(7);
        signal(L_CNT + 4, lane, nonce, rws, WS_FO + (rb * 16 + hp) * 4);
        STAMP(8);
        STAMP_FLUSH();
        return;
    }
    const int mw = wid;
    const unsigned abase = WS_ACT + (rt * 68 + mw) * 4096, wbase = WS_WT + (ct * 68 + mw) * 5120;
    unsigned vo = lane * 16;
    OPAQUE(vo);
    bf16x8 W[17][5], A[17][4];
#define LDW(i) _Pragma("unroll") for (int n = 0; n < 5; ++n) W[i][n] = ld128(rws, vo + (n & 3) * 1024, wbase + (i) * 20480 + (n >> 2) * 4096)
#define LDA(i) _Pragma("unroll") for (int t = 0; t < 4; ++t) A[i][t] = ld128(rws, vo + t * 1024, abase + (i) * 16384)
    STAMP(3);
    {
        unsigned vh[2][3];
#pragma unroll
        for (int i = 0; i < 3; ++i) { const int id = wid * 64 + lane + 512 * i + 256, rr = id / 12; vh[1][i] = rr * (3072 - 192) + c.cg * 192 + id * 16; }
        vh[0][0] = c.vh0; vh[0][1] = c.vh1; vh[0][2] = c.vh2;
#pragma unroll
        for (int t = 12; t < 15; ++t)
#pragma unroll
            for (int j = 0; j < 2; ++j) dma16(c.sh, vh[j][t % 3], (t / 3) * (128 * 3072), t * 8192 + wid * 1024 + j * 4096);
        const bf16x8 rt0 = cat(trread(c.brt), trread(c.brt + 2048)), rt1 = cat(trread(c.brt + 4096), trread(c.brt + 6144));
        if (lane < 8) *(LAS unsigned*)(uintptr_t)(L_CNT + lane * 4) = 0u;
        wait_bar_lgkm<6>();
        st32(nonce, rws, wid == 0 && lane == 0 ? 0 : OOB, WS_FQ + (c.rb * 16 + c.cg) * 4);
        asm volatile("s_waitcnt vmcnt(1)" ::: "memory");
        lds_count(L_CNT + 16, lane);
        st128(__builtin_bit_cast(u32x4, rt0), sws, vo, c.rd0);
        st128(__builtin_bit_cast(u32x4, rt1), sws, vo, c.rd1);
    }
    STAMP(4);
    {
        const int kind = (lane >> 1) & 3, dr = (lane >> 3) & 3;
        int rr = rb - ((kind == 1 || kind == 2) && dr < 3 ? dr : 0); rr = rr < 0 ? 0 : rr;
        const int cc = kind == 3 ? 15 : 5 * kind + ((sb + 2 * (lane & 1)) >> 3);
        const unsigned fo = WS_FQ + (rr * 16 + cc) * 4;
        unsigned v;
        asm volatile("global_load_dword %0, %1, %2 sc1\n\ts_waitcnt vmcnt(0)" : "=v"(v) : "v"(fo), "s"(ws) : "memory");
        signal(L_CNT + 20, lane, nonce, rws, WS_FW + blockIdx.x * 4);
        if (!__all(v == nonce)) poll(ws, fo, nonce);
    }
    STAMP(5);
    {
        u32x4 sg[21];
        unsigned tpo[3];
#pragma unroll
        for (int j = 0; j < 3; ++j) { int gtp = 4 * rb - 8 + 4 * j + mw; gtp = gtp < 0 ? 0 : gtp; tpo[j] = WS_QKV + (sb * 64 + gtp) * 1024; }
#pragma unroll
        for (int i = 0; i < 9; ++i) sg[i] = __builtin_bit_cast(u32x4, ld128(rws, vo, tpo[i % 3] + (40 + i / 3) * 65536));
#pragma unroll
        for (int i = 0; i < 3; ++i) sg[9 + i] = __builtin_bit_cast(u32x4, ld128(rws, vo, tpo[2] + i * 65536));
        const unsigned gt = __builtin_amdgcn_raw_buffer_load_b32(rws, (lane & 31) * 4, WS_G + (rb * 128 + mw * 32) * 4, SC1);
#pragma unroll
        for (int i = 0; i < 9; ++i) sg[12 + i] = __builtin_bit_cast(u32x4, ld128(rws, vo, tpo[i % 3] + (80 + i / 3) * 65536));
        SBAR();
        const unsigned la = mw * 1024 + lane * 16;
#pragma unroll
        for (int i = 0; i < 9; ++i) *(LAS u32x4*)(uintptr_t)(la + L_K + i * 4096) = sg[i];
#pragma unroll
        for (int i = 0; i < 3; ++i) *(LAS u32x4*)(uintptr_t)(la + L_Q + i * 4096) = sg[9 + i];
        *(LAS unsigned*)(uintptr_t)(L_GT + mw * 128 + (lane & 31) * 4) = gt;
        lds_count(L_CNT + 8, lane);
        asm volatile("s_waitcnt lgkmcnt(0)\n\ts_barrier" ::: "memory");
        STAMP(6);
        const bool one0 = odd && r == 0, one2 = !odd && r == 15;
#pragma unroll
        for (int i = 0; i < 9; ++i) {
            u32x4 v = sg[12 + i];
            if (i < 3 || i >= 6) {
#pragma unroll
                for (int e = 0; e < 4; ++e) v[e] = (i < 3 ? one0 : one2) ? 0x3f803f80u : v[e];
            }
            *(LAS u32x4*)(uintptr_t)(la + L_V + i * 4096) = v;
        }
    }
    lds_count(L_CNT + 12, lane);
    poll4(ws, WS_FW + vo, nonce);
    STAMP(29);
    if (blockIdx.x == 0 && wid == 0) st32((nonce - 0x7F4A7C15u) * 0x0E8B2F51u + 1u, rws, lane == 0 ? 0 : OOB, WS_E);
#pragma unroll
    for (int i = 0; i < 5; ++i) { LDW(i); }
    SBAR();
    OPAQUE(vo);
    poll(ws, WS_FH + (rt >> 1) * 64 + ((vo >> 2) & 60), nonce);
    STAMP(7);
    f32x4 acc[4][5];
    const f32x4 zero4 = {0.f, 0.f, 0.f, 0.f};
#define MMA(i) _Pragma("unroll") for (int n = 0; n < 5; ++n) _Pragma("unroll") for (int t = 0; t < 4; ++t) acc[t][n] = MFMA(W[i][n], A[i][t], (i) == 0 ? zero4 : acc[t][n])
    LDA(0); LDA(1); LDA(2); LDA(3);
    float gate[4];
    SBAR();
#pragma unroll
    for (int i = 0; i < 12; ++i) {
#pragma unroll
        for (int n = 0; n < 5; ++n) {
#pragma unroll
            for (int t = 0; t < 4; ++t) acc[t][n] = MFMA(W[i][n], A[i][t], i == 0 ? zero4 : acc[t][n]);
            SBAR();
            const int j = 5 * i + n + 23, ji = j / 5, jn = j % 5;
            if (j >= 25) W[ji][jn] = ld128(rws, vo + (jn & 3) * 1024, wbase + ji * 20480 + (jn >> 2) * 4096);
            SBAR();
        }
        if (i + 4 < 12) { LDA(i + 4); }
        if (i == 8) {
            OPAQUE(vo);
#pragma unroll
            for (int t = 0; t < 4; ++t) gate[t] = __uint_as_float(__builtin_amdgcn_raw_buffer_load_b32(rws, (vo >> 2) & 60, WS_G + (rt * 64 + t * 16) * 4, SC1));
        }
        SBAR();
    }
    W[16][3] = ld128(rws, vo + 3072, wbase + 16 * 20480); W[16][4] = ld128(rws, vo, wbase + 16 * 20480 + 4096);
#pragma unroll
    for (int t = 0; t < 4; ++t) {
        const float og = bfround(1.0f - gate[t]);
#pragma unroll
        for (int n = 0; n < 5; ++n) { acc[t][n] *= og; OPAQUE(acc[t][n]); }
    }
    STAMP(8);
    OPAQUE(vo);
    poll(ws, WS_FO + (rt >> 1) * 64 + ((vo >> 2) & 60), nonce);
    STAMP(9);
    LDA(12); LDA(13); LDA(14);
    A[15][0] = ld128(rws, vo, abase + 15 * 16384); A[15][1] = ld128(rws, vo + 1024, abase + 15 * 16384); SBAR();
    MMA(12); SBAR();
    A[15][2] = ld128(rws, vo + 2048, abase + 15 * 16384); A[15][3] = ld128(rws, vo + 3072, abase + 15 * 16384);
    LDA(16); SBAR();
    MMA(13); SBAR(); MMA(14); SBAR(); MMA(15); SBAR(); MMA(16); SBAR();
    STAMP(10);
    OPAQUE(vo);
#pragma unroll
    for (int t = 0; t < 4; ++t) {
        if (t != mw) {
            const int slot = mw > t ? mw - 1 : mw;
#pragma unroll
            for (int n = 0; n < 5; ++n) *(LAS f32x4*)(uintptr_t)(L_RED + ((t * 3 + slot) * 5 + n) * 1024 + vo) = acc[t][n];
        }
    }
    asm volatile("s_waitcnt lgkmcnt(0)\n\ts_barrier" ::: "memory");
    f32x4 fin[5];
#pragma unroll
    for (int n = 0; n < 5; ++n) fin[n] = mw == 0 ? acc[0][n] : mw == 1 ? acc[1][n] : mw == 2 ? acc[2][n] : acc[3][n];
#pragma unroll
    for (int s = 0; s < 3; ++s)
#pragma unroll
        for (int n = 0; n < 5; ++n) fin[n] += *(LAS f32x4*)(uintptr_t)(L_RED + ((mw * 3 + s) * 5 + n) * 1024 + vo);
    u16* op = (u16*)mkptr(ka[14], ka[15]) + (size_t)(rt * 64 + mw * 16 + ((vo >> 4) & 15)) * DM + ct * 80 + (vo >> 8) * 4;
#pragma unroll
    for (int n = 0; n < 5; ++n) *(GAS u32x2*)(op + n * 16) = (u32x2){pk2(fin[n][0], fin[n][1]), pk2(fin[n][2], fin[n][3])};
    STAMP(11);
    STAMP_FLUSH();
}

extern "C" void kernel_launch(void* const* d_in, const int* in_sizes, int n_in, void* d_out, int out_size, void* d_ws, size_t ws_size, hipStream_t stream) {
    fused_kernel<<<256, 512, 0, stream>>>((const u16*)d_in[0], (const u16*)d_in[1], (const u16*)d_in[4], (const u16*)d_in[5], (const u16*)d_in[2], (const u16*)d_in[6], (char*)d_ws, (u16*)d_out);
}
```

```cpp
#include <hip/hip_runtime.h>
#include <stdint.h>

typedef unsigned short u16;
typedef __attribute__((ext_vector_type(8))) __bf16 bf16x8;
typedef __attribute__((ext_vector_type(2))) __bf16 bf16x2;
typedef __attribute__((ext_vector_type(4))) float f32x4;
typedef __attribute__((ext_vector_type(2))) float f32x2;
typedef __attribute__((ext_vector_type(16))) float f32x16;
typedef __attribute__((ext_vector_type(4))) unsigned u32x4;
typedef __attribute__((ext_vector_type(2))) unsigned u32x2;
typedef __attribute__((ext_vector_type(16))) unsigned u32x16;
#define LAS __attribute__((address_space(3)))
#define GAS __attribute__((address_space(1)))
#define DI __device__ __forceinline__
typedef LAS char* lptr;
typedef __amdgpu_buffer_rsrc_t rsrc_t;

constexpr int SEQ = 2048, DM = 640, NQKV = 1920, DFF = 1536;
constexpr unsigned WS_QKV = 0;
constexpr unsigned WS_ACT = WS_QKV + 120 * 64 * 1024;
constexpr unsigned WS_WT = WS_ACT + 32 * 68 * 4096;
constexpr unsigned WS_G = WS_WT + 8 * 68 * 5120;
constexpr unsigned FS = 128;
constexpr unsigned WS_FQ = WS_G + 8192;
constexpr unsigned WS_FH = WS_FQ + 256 * FS;
constexpr unsigned WS_FO = WS_FH + 256 * FS;
constexpr unsigned WS_FW = WS_FO + 256 * FS;
constexpr unsigned WS_E = WS_FW + 256 * FS;
constexpr unsigned WS_END = WS_E + 256;
constexpr unsigned OOB = 0x7f000000u;
constexpr int SC1 = 16;

#define MPOLL 1
#define MFMA(a, b, c) __builtin_amdgcn_mfma_f32_16x16x32_bf16((a), (b), (c), 0, 0, 0)
#define MFMA32(a, b, c) __builtin_amdgcn_mfma_f32_32x32x16_bf16((a), (b), (c), 0, 0, 0)
#define SBAR() __builtin_amdgcn_sched_barrier(0)
#define OPAQUE(v) asm volatile("" : "+v"(v))

DI void op4(u32x4& v) { OPAQUE(v); }
DI void lds_inc(unsigned& old, unsigned cnt, unsigned val) { asm volatile("s_waitcnt vmcnt(0)\n\ts_mov_b64 exec, 1\n\tds_add_rtn_u32 %0, %1, %2\n\ts_mov_b64 exec, -1" : "+v"(old) : "v"(cnt), "v"(val) : "memory"); }
DI void lds_got(unsigned& old) { asm volatile("s_waitcnt lgkmcnt(0)" : "+v"(old) :: "memory"); }
DI void opf(float& v) { OPAQUE(v); }
DI unsigned pk2(float a, float b) { f32x2 v = {a, b}; return __builtin_bit_cast(unsigned, __builtin_convertvector(v, bf16x2)); }
DI float bfround(float a) { return __uint_as_float(pk2(a, 0.f) << 16); }
DI u32x4 make_srd(const void* p, unsigned bytes) {
    uint64_t a = (uint64_t)p; u32x4 r = {(unsigned)a, (unsigned)(a >> 32) & 0xffffu, bytes, 0x00020000u}; return r; }
DI void dma16(u32x4 srd, unsigned voff, unsigned soff, unsigned ldsaddr) {
    asm volatile("s_mov_b32 m0, %0\n\ts_nop 0\n\tbuffer_load_dwordx4 %1, %2, %3 offen lds" :: "s"(ldsaddr), "v"(voff), "s"(srd), "s"(soff) : "memory");
}
DI void dma4(u32x4 srd, unsigned voff, unsigned soff, unsigned ldsaddr) {
    asm volatile("s_mov_b32 m0, %0\n\ts_nop 0\n\tbuffer_load_dword %1, %2, %3 offen lds" :: "s"(ldsaddr), "v"(voff), "s"(srd), "s"(soff) : "memory");
}
DI void dma4s(u32x4 srd, unsigned voff, unsigned soff, unsigned ldsaddr) {
    asm volatile("s_mov_b32 m0, %0\n\ts_nop 0\n\tbuffer_load_dword %1, %2, %3 offen sc1 lds" :: "s"(ldsaddr), "v"(voff), "s"(srd), "s"(soff) : "memory");
}
typedef short v4i16_t __attribute__((ext_vector_type(4)));
DI u32x2 trread(unsigned a) { return __builtin_bit_cast(u32x2, __builtin_amdgcn_ds_read_tr16_b64_v4i16((LAS v4i16_t*)(uintptr_t)a)); }
DI bf16x8 cat(u32x2 a, u32x2 b) { u32x4 v = {a[0], a[1], b[0], b[1]}; return __builtin_bit_cast(bf16x8, v); }
DI bf16x8 ldsr128(unsigned a) { return *(LAS bf16x8*)(uintptr_t)a; }
DI u32x2 ldsr64(unsigned a) { return *(LAS u32x2*)(uintptr_t)a; }
template <int N> DI void wait_bar() { asm volatile("s_waitcnt vmcnt(%0)\n\ts_barrier" :: "n"(N) : "memory"); }
template <int N> DI void wait_bar_lgkm() { asm volatile("s_waitcnt vmcnt(%0) lgkmcnt(0)\n\ts_barrier" :: "n"(N) : "memory"); }
DI void st128(u32x4 v, u32x4 srd, unsigned voff, unsigned soff) { asm volatile("buffer_store_dwordx4 %0, %1, %2, %3 offen sc1\n\ts_nop 1" :: "v"(v), "v"(voff), "s"(srd), "s"(soff) : "memory"); }
DI void st32(unsigned v, rsrc_t rs, unsigned voff, unsigned soff) { __builtin_amdgcn_raw_buffer_store_b32(v, rs, voff, soff, SC1); }
DI bf16x8 ld128(rsrc_t rs, unsigned voff, unsigned soff) { return __builtin_bit_cast(bf16x8, __builtin_amdgcn_raw_buffer_load_b128(rs, voff, soff, SC1)); }
DI void poll(const char* ws, unsigned off, unsigned nonce) {
#pragma unroll 1
    for (int spin = 0; spin < (1 << 18); ++spin) {
        unsigned v;
        asm volatile("global_load_dword %0, %1, %2 sc1\n\ts_waitcnt vmcnt(0)" : "=v"(v) : "v"(off), "s"(ws) : "memory");
        if (v == nonce) break;
        __builtin_amdgcn_s_sleep(1);
    }
}

constexpr int SLOT = 32768, LDS_BYTES = 5 * SLOT;
constexpr int sbase(int n) { return (n % 5) * SLOT; }
constexpr int L_RT = 122880;
constexpr int L_GW = 4 * SLOT + 24576;
constexpr int L_CNT = 0, L_Q = 1024, L_GT = 13312, L_V = 14336, L_RED = 53248, L_K = 126976;
#define SB12 52

struct Ctx {
    u32x4 sx, sq, sh, s2, so;
    rsrc_t rws; u32x4 sws;
    unsigned vx0, vx1, vq0, vq1, vh0, vh1, vh2, vrt, srt;
    unsigned bx, bw, bh, brt;
    unsigned lane16;
    int wid, rb, cg, rot, par;
    unsigned vtc;
    unsigned nonce;
    unsigned rd0, rd1;
};
#define PAR 0
#define PRIOA 1
#define WEARLY 1
#define WPRE 0
#define NPK 2
#define MAP1 0
#define MAP2 0
#define MAP3 0
#define VN0 0
#define VPOS 0
#define FWT 5
#define FOI 9
#define HWT 1
#define HSD 4
#define PACE 0
#define GS 0
#define SSTEP 2
#define SS0 1
#define GPRIO 0
#define LEAD 20
#define FP1 5
#define FP2 99
#define PRIO2 1
#define PRIOH 0
#define LSLEEP 1
#define HSI 5
#define TOUCH 2
#define TSTR 310272
#define TPOS -1
#define CPF 1
#define ENDA 1
#define ROT 1
constexpr int nWait(int role, int ch, int m) {
    const int t = TPOS < 0 ? (m <= 2 ? TOUCH : 0) : (m == TPOS + 1 || m == TPOS + 2 ? TOUCH : 0);
    if (role != 2) return (m == 10 ? 9 : 8) + t;
    return (m == 0 ? 8 : m <= 4 ? 10 : m == 5 ? 8 : m == 6 ? 6 : m == 7 ? 4 : m == 8 ? 6 : 8) + t;
}
template <int ROLE, int CH> DI void bar_stage(int m) {
    if (m == 1) wait_bar<nWait(ROLE, CH, 1)>(); if (m == 2) wait_bar<nWait(ROLE, CH, 2)>(); if (m == 3) wait_bar<nWait(ROLE, CH, 3)>(); if (m == 4) wait_bar<nWait(ROLE, CH, 4)>();
    if (m == 5) wait_bar<nWait(ROLE, CH, 5)>(); if (m == 6) wait_bar<nWait(ROLE, CH, 6)>(); if (m == 7) wait_bar<nWait(ROLE, CH, 7)>(); if (m == 8) wait_bar<nWait(ROLE, CH, 8)>();
    if (m == 9) wait_bar_lgkm<nWait(ROLE, CH, 9)>(); if (m == 10) wait_bar<nWait(ROLE, CH, 10)>();
}
DI unsigned vhof(int id, int cg, int par) { const int r = id / 12; return (r ^ (par << 6)) * 3072 + (cg * 12 + id - r * 12) * 16; }
DI unsigned rt_doff(int rks, int n16) { const int ct = (n16 * 205) >> 10, nt = n16 - ct * 5; return WS_WT + ((ct * 68 + rks) * 5 + nt) * 1024; }

template <int ROLE> DI void issue_piece(const Ctx& c, int n, int i) {
    const unsigned b = sbase(n);
    if (TOUCH && TPOS >= 0 && n == TPOS + 3 && i == 0) dma4(c.wid >= 4 ? c.s2 : c.sh, c.vtc, c.wid >= 4 ? (ROLE == 2 ? OOB : c.srt - 128) : 0, b + c.wid * 2048);
    if (n < 10) {
        const int kr = n + 2 * c.rot, kb = (kr >= 10 ? kr - 10 : kr) ^ c.par;
        if (i == 0) dma16(c.sx, c.vx0, kb * 128, b + c.wid * 2048);
        if (i == 1) dma16(c.sx, c.vx1, kb * 128, b + c.wid * 2048 + 1024);
        if (ROLE != 2) {
            if (i == 2) dma16(c.sq, c.vq0, kb * (64 * 3840), b + 16384 + c.wid * 2048);
            if (i == 3) dma16(c.sq, c.vq0, kb * (64 * 3840) + 4 * 3840, b + 16384 + c.wid * 2048 + 1024);
        } else if (n == 3) {
            if (i == 2) dma16(c.sq, c.vq1, c.srt, b + 16384 + c.wid * 2048);
            if (i == 3) dma16(c.sq, c.vq1, c.srt + 80 * 1280, b + 16384 + c.wid * 2048 + 1024);
        }
    } else {
        const int t = (n - 10) * 4 + i;
        if (n == 12 && i == 0 && ROLE != 2) dma16(c.s2, c.vrt, c.srt, L_RT + c.wid * 1024);
        const int kr = t / 3 + c.rot, kb = kr >= 5 ? kr - 5 : kr;
        if (t < 12) dma16(c.sh, t % 3 == 0 ? c.vh0 : t % 3 == 1 ? c.vh1 : c.vh2, kb * (128 * 3072), t * 8192 + c.wid * 1024);
    }
}
DI bf16x8 rd_wq(unsigned bw, int J, int u) {
    const int k = 2 * J + (u >> 1);
    const unsigned a = bw ^ (unsigned)(((u & 1) << 6) | sbase(k >> 2));
    return cat(trread(a + (k & 3) * 4096), trread(a + (k & 3) * 4096 + 1024));
}
#define KTV 30
constexpr int KT = KTV;
constexpr int p2k(int s) { return s < 3 * KT ? s / 3 : KT + (s - 3 * KT) % (40 - KT); }
constexpr int p2v(int s) { return s < 3 * KT ? s % 3 : (s - 3 * KT) / (40 - KT); }
DI bf16x8 rd_wh(unsigned bh, int s) {
    const int k = p2k(s), v = p2v(s);
    const unsigned a = bh + (k >= 20 ? 61440 : 0);
    const int o = (k % 20) * 3072 + v * 64;
    return cat(trread(a + o), trread(a + o + 768));
}
DI bf16x8 rd_x(unsigned bx, int k) { return ldsr128(bx ^ (unsigned)(((k & 3) << 5) | sbase(k >> 2))); }
DI void retile1(const Ctx& c, unsigned a, unsigned doff) {
    const u32x2 a0 = trread(a), b0 = trread(a + 2048);
    st128((u32x4){a0[0], a0[1], b0[0], b0[1]}, c.sws, c.lane16, doff);
}
typedef short i16x2 __attribute__((ext_vector_type(2)));
DI unsigned relu2(unsigned v) { const i16x2 z = {0, 0}; return __builtin_bit_cast(unsigned, __builtin_elementwise_max(__builtin_bit_cast(i16x2, v), z)); }
DI u32x4 pk8(const f32x16& a, int i, int j) { return (u32x4){pk2(a[i], a[i + 1]), pk2(a[i + 2], a[i + 3]), pk2(a[j], a[j + 1]), pk2(a[j + 2], a[j + 3])}; }

template <int N> DI void signal(unsigned cnt, int lane, unsigned nonce, rsrc_t rws, unsigned flag) {
    asm volatile("s_waitcnt vmcnt(%0)" :: "n"(N) : "memory");
    if (lane == 0) {
        unsigned old;
        asm volatile("ds_add_rtn_u32 %0, %1, %2\n\ts_waitcnt lgkmcnt(0)" : "=v"(old) : "v"(cnt), "v"(1) : "memory");
        if (old == 3) st32(nonce, rws, 0, flag);
    }
}
DI void lds_count(unsigned cnt, int lane) { if (lane == 0) asm volatile("s_waitcnt lgkmcnt(0)\n\tds_add_u32 %0, %1" :: "v"(cnt), "v"(1) : "memory"); }
DI void lds_add(unsigned cnt) { asm volatile("s_mov_b64 exec, 1\n\tds_add_u32 %0, %1\n\ts_mov_b64 exec, -1" :: "v"(cnt), "v"(1) : "memory"); }
DI void lds_wait(unsigned cnt) {
#pragma unroll 1
    for (int spin = 0; spin < (1 << 20); ++spin) {
        unsigned v;
        asm volatile("ds_read_b32 %0, %1\n\ts_waitcnt lgkmcnt(0)" : "=v"(v) : "v"(cnt) : "memory");
        if (__builtin_amdgcn_readfirstlane(v) == 4) break;
        if (LSLEEP) __builtin_amdgcn_s_sleep(LSLEEP);
    }
}
template <int I> struct IC { static constexpr int v = I; };
template <int A, int B, class F> DI void sfor(F&& f) { if constexpr (A < B) { f(IC<A>{}); sfor<A + 1, B>(f); } }

template <int ROLE, int CH>
DI void phase12(Ctx& c, char* ws) {
    constexpr int NW = 5;
    bf16x8 xf[40], wf[NW];
    f32x16 acc[2];
    const f32x16 zero16 = {0.f, 0.f, 0.f, 0.f, 0.f, 0.f, 0.f, 0.f, 0.f, 0.f, 0.f, 0.f, 0.f, 0.f, 0.f, 0.f};
    const int tq = c.wid & 3;
    OPAQUE(c.lane16);
    const int lane = c.lane16 >> 4;
    if (TOUCH && TPOS < 0) dma4(CH ? c.s2 : c.sh, c.vtc, CH ? (ROLE == 2 ? OOB : c.srt - 128) : 0, sbase(4) + c.wid * 2048);
    if (TOUCH > 1) dma4s(c.wid == 1 ? c.so : c.sws, c.wid < 2 ? lane * TSTR : OOB, 0, sbase(4) + c.wid * 2048);
    unsigned e;
    asm volatile("s_load_dword %0, %1, %3\n\ts_waitcnt vmcnt(%2) lgkmcnt(0)\n\ts_barrier" : "=s"(e) : "s"(ws), "n"(nWait(ROLE, CH, 0)), "s"(WS_E) : "memory");
    c.nonce = e * 0x9E3779B1u + 0x7F4A7C15u;
    if (ROLE != 2) {
        xf[0] = rd_x(c.bx, 0); xf[1] = rd_x(c.bx, 1);
#pragma unroll
        for (int u = 0; u < 4; ++u) wf[u] = rd_wq(c.bw, 0, u);
        SBAR();
#pragma unroll
        for (int J = 0; J < 20; ++J) {
            OPAQUE(c.bx); OPAQUE(c.bw);
#pragma unroll
            for (int u = 0; u < 4; ++u) {
                const int k = 2 * J + (u >> 1), v = u & 1;
                const f32x16 ci = k == 0 ? zero16 : acc[v];
                acc[v] = ROLE == 1 ? MFMA32(xf[k], wf[u], ci) : MFMA32(wf[u], xf[k], ci);
                SBAR();
                if (u == 0 && (J & 1)) {
                    bar_stage<ROLE, CH>((J + 1) >> 1);
                }
                if (u < 2 && J < 19) xf[2 * J + 2 + u] = rd_x(c.bx, 2 * J + 2 + u);
                if (J < 19) wf[u] = rd_wq(c.bw, J + 1, u);
                else if (CH) { wf[u] = rd_wh(c.bh, u); if (u == 3) wf[4] = rd_wh(c.bh, 4); }
                if (J == 0) issue_piece<ROLE>(c, 3, u);
                else if (u >= 2) issue_piece<ROLE>(c, ((J + 1) >> 1) + 3, (u - 2) + ((J & 1) ? 0 : 2));
                SBAR();
            }
        }
    } else {
        f32x4 z0 = {0.f, 0.f, 0.f, 0.f}, z1 = z0;
        const unsigned ga = L_GW + (((lane ^ (lane >> 4)) & 1) ? 2048 : (lane >> 5) * 16);
#pragma unroll
        for (int m = 0; m <= 10; ++m) {
            bar_stage<2, CH>(m);
#pragma unroll
            for (int i = 0; i < 4; ++i) issue_piece<2>(c, m + 3, i);
            if (m < 10) {
                OPAQUE(c.bx);
#pragma unroll
                for (int i = 0; i < 4; ++i) xf[4 * m + i] = rd_x(c.bx, 4 * m + i);
                if (m < 4) {
                    const int l = m * 4 + (c.wid >> 1), id = c.rb * 3 + (c.wid >> 1);
                    const bool valid = m < 3 ? l < 10 : (c.wid >> 1) < 3 && id < 40;
                    const int rks = m < 3 ? 48 + c.rb : 64 + (id & 3), n16 = 4 * (m < 3 ? l : id >> 2) + (c.wid & 1) * 2;
                    retile1(c, c.bw + sbase(m), valid ? rt_doff(rks, n16) : OOB);
                    retile1(c, c.bw + sbase(m) + 32, valid ? rt_doff(rks, n16 + 1) : OOB);
                }
                if (CH == 0) {
#pragma unroll
                    for (int i = 0; i < 4; i += 2) { z0 = MFMA(ldsr128(ga + (4 * m + i) * 32), xf[4 * m + i], z0); z1 = MFMA(ldsr128(ga + (4 * m + i + 1) * 32), xf[4 * m + i + 1], z1); }
                } else asm volatile("s_waitcnt lgkmcnt(0)" ::: "memory");
            }
            SBAR();
        }
        if (CH) {
#pragma unroll
            for (int u = 0; u < NW; ++u) wf[u] = rd_wh(c.bh, u);
        } else {
            z0 += z1;
            const float z = bfround((lane & 16) ? z0[1] : z0[0]);
            const float gt = bfround(1.0f / (1.0f + __expf(-z)));
            st32(__float_as_uint(gt), c.rws, lane < 32 ? lane * 4 : OOB, WS_G + (c.rb * 128 + tq * 32) * 4);
        }
    }
    if (ROLE == 0) {
        const float qs = __uint_as_float(c.cg < 5 ? __float_as_uint(0.22360679774997896f * 1.4426950408889634f) : 0x3f800000u);
        acc[0] *= qs; acc[1] *= qs;
    }
    const unsigned so = WS_QKV + ((8 * c.cg + 4 * CH) * 64 + 4 * c.rb + tq) * 1024;
    const unsigned vst = ROLE == 0 ? c.lane16 : ((lane >> 4) & 1) * 65536 + (lane >> 5) * 256 + (lane & 15) * 16;
    constexpr unsigned sd = ROLE == 0 ? 65536 : 512;
    if (CH == 0) {
        if (ROLE != 2) {
            if (GS) { SBAR(); __builtin_amdgcn_s_sleep(GS); SBAR(); }
#pragma unroll
            for (int v = 0; v < 2; ++v) { st128(pk8(acc[v], 0, 4), c.sws, vst, so + v * 131072); st128(pk8(acc[v], 8, 12), c.sws, vst, so + v * 131072 + sd); }
        }
        wait_bar<ROLE != 2 ? 8 : 5>();
        return;
    }
    __builtin_amdgcn_s_setprio(PRIO2);
    f32x16 hc[3];
    u32x4 pq[4];
    unsigned cw;
    const int tt = 8 * c.rb + 2 * tq;
    const unsigned sa = WS_ACT + (((tt >> 2) * 68 + 3 * c.cg) * 4 + (tt & 3)) * 1024;
    const unsigned vo = ((lane >> 4) & 1) * 1024 + (lane >> 5) * 256 + (lane & 15) * 16;
    auto hstore = [&](int v, int half) __attribute__((always_inline)) {
        u32x4 hv = pk8(hc[v], half * 4, half * 4 + 8);
#pragma unroll
        for (int i = 0; i < 4; ++i) hv[i] = relu2(hv[i]);
        if (PRIOH) { SBAR(); __builtin_amdgcn_s_setprio(PRIOH); }
        st128(hv, c.sws, vo, sa + v * 4096 + half * 512);
        if (PRIOH) { __builtin_amdgcn_s_setprio(PRIO2); SBAR(); }
    };
    constexpr int SW = 3 * KT + (32 - KT) - NW;
    sfor<0, 120>([&](auto S) __attribute__((always_inline)) {
        constexpr int s = S.v, k = p2k(s), v = p2v(s);
        hc[v] = MFMA32(wf[s % NW], xf[k], k == 0 ? zero16 : hc[v]);
        SBAR();
        if (s == 10) { constexpr int ns = SS0 >= 10 ? 0 : (9 - SS0) / SSTEP + 1; wait_bar<ROLE != 2 ? 4 + (ns > 4 ? 4 : ns) : 4>(); }
        if (s == SB12) { wait_bar<0>(); }
        if (SB12 < 52 && s == 56) lds_add(L_CNT + 20);
        if (s == SW - 4) cw = *(volatile LAS unsigned*)(uintptr_t)(L_CNT + 16);
        if (s == SW) { if (__builtin_amdgcn_readfirstlane(cw) != 4) lds_wait(L_CNT + 16); }
        if (s + NW < 120) wf[s % NW] = rd_wh(c.bh, s + NW);
        if constexpr (s >= 3 * KT + (40 - KT) && (k == KT + 2 || k == KT + 5)) hstore(v - 1, k == KT + 5);
        if (ROLE != 2) {
            if constexpr (s < 2) { pq[2 * s] = pk8(acc[s], 0, 4); pq[2 * s + 1] = pk8(acc[s], 8, 12); }
            if constexpr (s >= 30 && s < 34) { OPAQUE(c.bx); xf[s + 6] = rd_x(c.bx, s + 6); }
            if constexpr (s >= SS0 && s < SS0 + 4 * SSTEP && (s - SS0) % SSTEP == 0) { constexpr int q = (s - SS0) / SSTEP; st128(pq[q], c.sws, vst, so + (q >> 1) * 131072 + (q & 1) * sd); }
        }
        SBAR();
    });
    hstore(2, 0); hstore(2, 1);
}

DI float max3f(float a, float b, float c) { return fmaxf(fmaxf(a, b), c); }
DI const void* mkptr(unsigned lo, unsigned hi) { return (const void*)(((uint64_t)hi << 32) | lo); }

__global__ __launch_bounds__(512, 2) void fused_kernel(const u16* x_, const u16* wqkv_, const u16* w1_, const u16* w2_, const u16* wo_, const u16* gw_, char* ws_, u16* out_) {
    __shared__ __attribute__((aligned(16))) char smem[LDS_BYTES];
    Ctx c;
    u32x16 ka;
    asm volatile("s_load_dwordx16 %0, %1, 0x0" : "=s"(ka) : "s"(__builtin_amdgcn_kernarg_segment_ptr()) : "memory");
    const int xcd = blockIdx.x & 7, idx = blockIdx.x >> 3;
    {
        u32x2 pc; unsigned len, vc;
        asm volatile("s_getpc_b64 %0\n.Lfk_pc:\n\ts_mov_b32 %1, .Lfk_end-.Lfk_pc\n\tv_lshl_add_u32 %2, %3, 12, %4" : "=s"(pc), "=s"(len), "=v"(vc) : "v"(threadIdx.x), "s"(idx * 128));
        dma4((u32x4){pc[0], pc[1] & 0xffffu, len, 0x00020000u}, vc, 0, sbase(4));
    }
    const int wid = __builtin_amdgcn_readfirstlane(threadIdx.x >> 6);
    char* ws;
    {
        const int lane = threadIdx.x & 63;
        const int r = lane & 15, g = lane >> 4, q4 = r >> 2, p4 = r & 3;
        const int rb = MAP1 == 0 ? (xcd & 1) * 8 + (idx & 7) : MAP1 == 1 ? 2 * xcd + (idx & 1) : (xcd >> 1) * 4 + (idx & 3);
        const int cg = MAP1 == 0 ? (xcd >> 1) * 4 + (idx >> 3) : MAP1 == 1 ? idx >> 1 : (xcd & 1) * 8 + (idx >> 2);
        const bool r2 = cg == 15;
        c.wid = wid; c.rb = rb; c.cg = cg; c.lane16 = lane * 16;
        { const int q = ROT == 1 ? (idx & 7) + cg : (xcd >> 1) + 2 * (xcd & 1) + (idx & 1), qm = ROT == 1 ? q - ((q * 52) >> 8) * 5 : q >= 5 ? q - 5 : q; c.rot = !ROT || r2 ? 0 : qm; }
        c.par = PAR && !r2 ? (idx ^ (idx >> 3)) & 1 : 0;
        const int t0 = 16 * wid + (lane >> 3), t1 = t0 + 8;
        c.vx0 = (rb * 128 + t0) * 1280 + (((lane & 7) ^ ((t0 >> 1) & 7)) << 4);
        c.vx1 = (rb * 128 + t1) * 1280 + (((lane & 7) ^ ((t1 >> 1) & 7)) << 4);
        {
            const unsigned qa = (8 * wid + g) * 3840 + cg * 256 + ((r ^ (g << 2)) << 4);
            const unsigned qb = (40 * rb + 8 * (wid & 1) + (lane >> 3)) * 1280 + (lane & 7) * 16 + (wid >> 1) * 128;
            c.vq0 = r2 ? qb : qa;
        }
        asm volatile("s_waitcnt lgkmcnt(0)" : "+s"(ka), "+v"(c.vx0), "+v"(c.vx1), "+v"(c.vq0) :: "memory");
        asm volatile("" :: "v"((LAS char*)smem) : "memory");
        c.sx = make_srd(mkptr(ka[0], ka[1]), SEQ * DM * 2);
        c.sq = make_srd(mkptr(r2 ? ka[8] : ka[2], r2 ? ka[9] : ka[3]), r2 ? DM * DM * 2 : DM * NQKV * 2);
        unsigned swm = r2 ? 512 : 64 * 3840, swa = r2 ? 20 * 1280 : 4 * 3840;
        asm volatile("" : "+s"(swm), "+s"(swa));
        dma16(make_srd(mkptr(ka[10], ka[11]), r2 ? 1280 : 0), lane * 16, wid * 1024, L_GW + wid * 1024);
#pragma unroll
        for (int n = 0; n < 3; ++n) {
            const int kr = n + 2 * c.rot, kb = (n == 2 && kr >= 10 ? 0 : kr) ^ c.par;
            const unsigned b = sbase(n) + wid * 2048, sw = kb * swm;
            dma16(c.sx, c.vx0, kb * 128, b); dma16(c.sq, c.vq0, sw, b + 16384);
            dma16(c.sx, c.vx1, kb * 128, b + 1024); dma16(c.sq, c.vq0, sw + swa, b + 16384 + 1024);
        }
        SBAR();
        ws = (char*)mkptr(ka[12], ka[13]);
        c.sh = make_srd(mkptr(ka[4], ka[5]), DM * DFF * 2); c.s2 = make_srd(mkptr(ka[6], ka[7]), DFF * DM * 2);
        c.sws = make_srd(ws, WS_END); c.so = make_srd(mkptr(ka[14], ka[15]), SEQ * DM * 2);
        c.rws = __builtin_amdgcn_make_buffer_rsrc(ws, 0, WS_END, 0x00020000);
        const int id0 = wid * 64 + lane, id1 = id0 + 512, id2 = id0 + 1024;
        c.vh0 = vhof(id0, cg, c.par); c.vh1 = vhof(id1, cg, c.par); c.vh2 = vhof(id2, cg, c.par);
        { const int l = (wid & 3) * 15 + lane, rw = (l * 43) >> 8;
          c.vtc = wid < 4 ? (lane < 15 ? (idx * 20 + (xcd & 1) * 10 + rw) * 3072 + (xcd >> 1) * 768 + (l - rw * 6) * 128 : OOB) : 0; }
        const int cc = lane & 31, h = lane >> 5;
        c.bx = (wid & 3) * 4096 + cc * 128 + ((h ^ ((cc >> 1) & 7)) << 4);
        c.bh = (8 * h + q4) * 192 + (g & 1) * 32 + 8 * p4;
        if (cg < 15) {
            c.bw = 16384 + (8 * h + q4) * 256 + (((2 * (wid >> 2)) ^ q4) << 6) + (g & 1) * 32 + 8 * p4;
            const int blk = (rb * 15 + cg) * 2 + (wid >> 2), rks = blk / 10, rcl = blk - rks * 10;
            c.vrt = (lane >> 3) * 1280 + (lane & 7) * 16; c.srt = (rks * 32 + (wid & 3) * 8) * 1280 + rcl * 128;
            if (wid >= 4) c.vtc = lane < 16 ? (lane & 7) * 1280 + (lane >> 3) * 128 : OOB;
            c.brt = L_RT + (4 * g + q4) * 128 + (wid & 3) * 32 + 8 * p4;
            c.vq1 = 0; c.rd0 = rt_doff(rks, 4 * rcl + (wid & 3)); c.rd1 = rt_doff(rks, 4 * rcl + 4 + (wid & 3));
            if (cg < 10) { if (wid < 4) phase12<0, 0>(c, ws); else phase12<0, 1>(c, ws); }
            else { if (wid < 4) phase12<1, 0>(c, ws); else phase12<1, 1>(c, ws); }
        } else {
            const int hf = wid & 1, rho = lane >> 3;
            c.vq1 = (40 * (rho >> 2) + 20 * hf + 16 + (rho & 3)) * 1280 + (lane & 7) * 16;
            const int id = rb * 3 + (wid >> 1);
            c.srt = (wid >> 1) < 3 && id < 40 ? (id & 3) * 160 * 1280 + (id >> 2) * 128 : OOB;
            c.bw = 16384 + (wid >> 1) * 4096 + (4 * g + q4) * 128 + (wid & 1) * 64 + 8 * p4;
            c.vrt = 0; c.brt = L_RT; c.rd0 = OOB; c.rd1 = OOB;
            if (wid < 4) phase12<2, 0>(c, ws); else phase12<2, 1>(c, ws);
        }
    }
    OPAQUE(c.lane16);
    const int lane = c.lane16 >> 4, r = lane & 15;
    const rsrc_t rws = c.rws; const u32x4 sws = c.sws;
    const unsigned nonce = __builtin_amdgcn_readfirstlane(c.nonce);
    const int hp = MAP2 == 0 ? 2 * xcd + (idx & 1) : MAP2 == 1 ? idx >> 1 : (xcd & 1) * 8 + (idx >> 2);
    const int rb = MAP2 == 0 ? idx >> 1 : MAP2 == 1 ? 2 * xcd + (idx & 1) : (xcd >> 1) * 4 + (idx & 3);
    const int rt = MAP3 == 1 ? 4 * xcd + (idx & 3) : (xcd >> 1) * 8 + (idx & 7), ct = MAP3 == 1 ? idx >> 2 : (xcd & 1) * 4 + (idx >> 3);
    const int sb = (5 * hp) >> 1, odd = hp & 1;
    if (wid >= 4) {
        const int wq = wid - 4;
        const int qc = lane & 31, h = lane >> 5;
        unsigned dm[8];
#pragma unroll
        for (int i = 0; i < 8; ++i) { const int kk = 8 * (i >> 1) + 4 * h + 2 * (i & 1); dm[i] = (kk <= qc ? 0xffffu : 0u) | (kk + 1 <= qc ? 0xffff0000u : 0u); }
        const int tfirst = 8 - 4 * rb - wq;
        const f32x16 zero16 = {0.f, 0.f, 0.f, 0.f, 0.f, 0.f, 0.f, 0.f, 0.f, 0.f, 0.f, 0.f, 0.f, 0.f, 0.f, 0.f};
        const float NEG = -1e30f;
        float cap[8];
        unsigned one1[8];
#pragma unroll
        for (int T = 0; T < 8; ++T) { one1[T] = T && tfirst > T ? 0x3f803f80u : 0x3f80u; asm volatile("" : "+s"(one1[T])); }
#pragma unroll
        for (int T = 0; T < 8; ++T) { unsigned cu = tfirst > T ? 0xf149f2cau : (T ? 0x7f800000u : 0u); asm volatile("" : "+s"(cu)); cap[T] = __uint_as_float(cu); }
        const int tt = 8 * rb + 2 * wq;
        unsigned obase = WS_ACT + ((tt >> 2) * 68 * 4 + (tt & 3)) * 1024;
        unsigned ost0 = obase + (48 + hp) * 4096, ost1 = obase + (64 + (hp >> 2)) * 4096 + (hp & 3) * 256;
        unsigned vo0 = (qc >> 4) * 1024 + h * 256 + (qc & 15) * 16, qm = 0;
        unsigned kp = L_K + wq * 1024 + lane * 16, qp = L_Q + wq * 1024 + lane * 16, vp[2];
#pragma unroll
        for (int hh = 0; hh < 2; ++hh) {
            const int fb = odd * 8 + 4 * hh, f = qc < 20 ? 16 * hh + fb + qc : (odd ? 0 : 47);
            vp[hh] = L_V + wq * 1024 + (f >> 4) * 12288 + h * 256 + (f & 15) * 16;
            qm |= ((4 * h >= fb ? 1 : 0) | (8 + 4 * h >= fb ? 2 : 0) | (4 * h < fb + 4 ? 4 : 0) | (8 + 4 * h < fb + 4 ? 8 : 0)) << (4 * hh);
        }
        f32x16 sc[2][9], o[2];
#pragma unroll
        for (int e = 0; e < 16; ++e) sc[0][0][e] = cap[0];
        asm volatile("" : "+v"(sc[0][0]), "+v"(kp), "+v"(qp), "+v"(vp[0]), "+v"(vp[1]), "+v"(qm), "+v"(vo0));
        unsigned ost2 = ost0 + 512;
        asm volatile("" : "+s"(ost0), "+s"(ost1), "+s"(ost2));
        asm volatile("" : "+v"(dm[0]), "+v"(dm[1]), "+v"(dm[2]), "+v"(dm[3]), "+v"(dm[4]), "+v"(dm[5]), "+v"(dm[6]), "+v"(dm[7]));
        bool hsig;
        const unsigned fh = WS_FH + (c.rb * 16 + c.cg) * FS;
        {
            unsigned v;
            asm volatile("ds_read_b32 %0, %1 offset:8\n\ts_waitcnt lgkmcnt(0)" : "=v"(v) : "v"(L_CNT) : "memory");
            hsig = __builtin_amdgcn_readfirstlane(v) != 4;
            if (hsig) signal<0>(L_CNT, lane, nonce, rws, fh);
        }
        unsigned hold = 0;
        if (hsig) lds_wait(L_CNT + 8);
        __builtin_amdgcn_s_setprio(PRIOA);
        bf16x8 kf[2][9][2], qf[2][2], vf[2][9][2];
        float mxa[2], mxb[2], mxc[2], ta, tb, tc, td, gate;
        bf16x8 nm;
        u32x4 av = {0x3f803f80u, 0u, 0u, 0u};
        unsigned p[2][4], pd[4], orem[2];
        constexpr int ord[9] = {8, 0, 1, 2, 3, 4, 5, 6, 7};
        auto ldk = [&](int hh, int i, int j) __attribute__((always_inline)) { kf[hh][i][j] = ldsr128(kp + (hh + j) * 12288 + ord[i] * 1024); };
        auto ldv = [&](int hh, int T, int j) __attribute__((always_inline)) { vf[hh][T][j] = ldsr128(vp[hh] + T * 1024 + j * 512); };
        auto ldq = [&](int hh) __attribute__((always_inline)) {
#pragma unroll
            for (int j = 0; j < 2; ++j) {
                const u32x4 q = __builtin_bit_cast(u32x4, ldsr128(qp + (hh + j) * 4096));
                const unsigned m0 = __builtin_amdgcn_sbfe(qm, 4 * hh + 2 * j, 1), m1 = __builtin_amdgcn_sbfe(qm, 4 * hh + 2 * j + 1, 1);
                qf[hh][j] = __builtin_bit_cast(bf16x8, (u32x4){q[0] & m0, q[1] & m0, q[2] & m1, q[3] & m1});
            }
        };
        auto qk = [&](int hh, int i, int j) __attribute__((always_inline)) {
            const int T = ord[i];
            sc[hh][T] = MFMA32(kf[hh][i][j], qf[hh][j], (j == 0 && T != 0) ? zero16 : sc[hh][T]);
        };
        auto merge = [&](int hh, int j) __attribute__((always_inline)) {
#pragma unroll
            for (int e = 8 * j; e < 8 * j + 8; ++e) sc[hh][0][e] = 8 * (e >> 2) + 4 * h + (e & 3) <= qc ? sc[hh][8][e] : sc[hh][0][e];
        };
        auto tmax = [&](int hh, int T, int j) __attribute__((always_inline)) {
            const f32x16& x = sc[hh][T];
            if (j == 0) { ta = max3f(x[0], x[1], x[2]); tb = max3f(x[3], x[4], x[5]); tc = max3f(x[6], x[7], x[8]); td = max3f(x[9], x[10], x[11]); opf(ta); opf(tb); opf(tc); opf(td); }
            else {
                float f = max3f(x[12], x[13], x[14]);
                ta = max3f(ta, tb, x[15]); opf(f); opf(ta);
                if (T == 0) { tc = max3f(tc, td, f); opf(tc); mxa[hh] = __builtin_amdgcn_fmed3f(ta, tc, __builtin_inff()); }
                else { tc = max3f(tc, td, f); mxb[hh] = __builtin_amdgcn_fmed3f(T == 1 ? NEG : mxb[hh], ta, cap[T]); mxc[hh] = __builtin_amdgcn_fmed3f(T == 1 ? NEG : mxc[hh], tc, cap[T]); }
            }
        };
        auto fin = [&](int hh) __attribute__((always_inline)) {
            float m = max3f(mxa[hh], mxb[hh], mxc[hh]);
            auto sw = __builtin_amdgcn_permlane32_swap(__float_as_uint(m), __float_as_uint(m), false, false);
            const float x = -fmaxf(__uint_as_float(sw[0]), __uint_as_float(sw[1]));
            const float r1 = x - __uint_as_float(__float_as_uint(x) & 0xffff0000u), r2 = r1 - __uint_as_float(__float_as_uint(r1) & 0xffff0000u);
            const unsigned b0 = __builtin_amdgcn_perm(__float_as_uint(r1), __float_as_uint(x), 0x07060302u), b1 = (__float_as_uint(r2) >> 16) | 0xf1490000u;
            const bool lo = !(vo0 & 256);
            nm = __builtin_bit_cast(bf16x8, (u32x4){lo ? b0 : 0u, lo ? b1 : 0u, 0u, 0u});
        };
        auto shift = [&](int hh, int T) __attribute__((always_inline)) {
            av[1] = one1[T]; op4(av);
            sc[hh][T] = MFMA32(__builtin_bit_cast(bf16x8, av), nm, sc[hh][T]);
        };
        auto soft = [&](int hh, int T, int j, int q) __attribute__((always_inline)) {
#pragma unroll
            for (int e = 2 * q; e < 2 * q + 2; ++e) p[j][e] = pk2(__builtin_amdgcn_exp2f(sc[hh][T][8 * j + 2 * e]), __builtin_amdgcn_exp2f(sc[hh][T][8 * j + 2 * e + 1]));
        };
        auto pv = [&](int hh, int T, int j) __attribute__((always_inline)) {
            if (T == 0) {
#pragma unroll
                for (int e = 0; e < 4; ++e) { pd[e] = p[j][e] & dm[4 * j + e]; p[j][e] ^= pd[e]; }
                o[hh] = MFMA32(vf[hh][8][j], __builtin_bit_cast(bf16x8, (u32x4){pd[0], pd[1], pd[2], pd[3]}), j == 0 ? zero16 : o[hh]);
            }
            o[hh] = MFMA32(vf[hh][T][j], __builtin_bit_cast(bf16x8, (u32x4){p[j][0], p[j][1], p[j][2], p[j][3]}), o[hh]);
        };
        auto outp = [&](int hh) __attribute__((always_inline)) {
            const float cf = gate * __builtin_amdgcn_rcpf(o[hh][12]);
            f32x16 w = o[hh] * cf;
            st128(pk8(w, 0, 4), sws, vo0, hh ? ost2 : ost0);
            const unsigned r0 = pk2(w[8], w[9]), r1 = pk2(w[10], w[11]);
            if (hh == 0) { orem[0] = r0; orem[1] = r1; }
            else st128((u32x4){orem[0], orem[1], r0, r1}, sws, (vo0 & 256) ? OOB : vo0, ost1);
        };
        ldk(0, 0, 1); ldk(0, 0, 0); ldq(0); ldk(0, 2, 1); ldk(0, 2, 0); ldk(0, 1, 1); ldk(0, 1, 0);
        SBAR();
        sfor<0, 22>([&](auto S) __attribute__((always_inline)) {
            constexpr int u = S.v, i = u >> 1, j = u & 1;
            if constexpr (i < 9) qk(0, i, j);
            if constexpr ((u & 3) == 0 && i + 3 < 9) { ldk(0, i + 4, 1); ldk(0, i + 4, 0); }
            if constexpr ((u & 3) == 1 && i + 3 < 9) { ldk(0, i + 3, 1); ldk(0, i + 3, 0); }
            if constexpr (u == 5 || u == 6) merge(0, u - 5);
            if constexpr (u == 7 || u == 8) tmax(0, 0, u - 7);
            if constexpr (u >= 8) tmax(0, (u - 8) / 2 + 1, j);
            if constexpr (u == 2 * HSI) lds_inc(hold, hsig ? L_CNT + 24 : L_CNT, 1);
            if constexpr (u == 2 * HSI + HSD) { lds_got(hold); st32(nonce, rws, hold == (hsig ? 99 : 3) ? 0 : OOB, fh); }
            SBAR();
        });
        fin(0); shift(0, 0);
        lds_wait(L_CNT + 12);
        ldk(1, 0, 1); ldk(1, 0, 0); ldq(1); ldk(1, 1, 1); ldk(1, 1, 0); ldv(0, 0, 1); ldv(0, 8, 1); ldv(0, 0, 0); ldv(0, 8, 0);
        SBAR();
        sfor<0, 33>([&](auto S) __attribute__((always_inline)) {
            constexpr int g = S.v, hh = g >> 4, u = g & 15, T = u >> 1, j = u & 1;
            constexpr int gp = g - 1, hq = gp >> 4, Tp = (gp & 15) >> 1, jp = gp & 1;
            constexpr int qa = g == 0 ? 0 : g == 1 ? 2 : g + 2, qn = g < 2 ? 2 : 1;
            if constexpr (g == 16) { gate = *(LAS float*)(uintptr_t)(L_GT + wq * 128 + ((vo0 >> 4) & 64) + ((vo0 >> 2) & 60)); pv(0, 7, 1); tmax(1, 7, 0); tmax(1, 7, 1); fin(1); shift(1, 0); SBAR(); outp(0); SBAR(); }
            if constexpr (g < 32) soft(hh, T, j, 0);
            if constexpr (g == 1) {
#pragma unroll
                for (int e = 0; e < 16; ++e) sc[1][0][e] = cap[0];
            }
            SBAR();
            if constexpr (g > 0 && g != 16) pv(hq, Tp, jp);
            if constexpr (j == 1) {
                if constexpr (hh == 0 && T + 3 < 9) ldk(1, T + 3, 0);
                if constexpr (hh == 0 && T + 2 < 9) ldk(1, T + 2, 1);
                if constexpr (T < 7) { ldv(hh, T + 1, 1); ldv(hh, T + 1, 0); }
                else if constexpr (hh == 0) { ldv(1, 0, 1); ldv(1, 8, 1); ldv(1, 0, 0); ldv(1, 8, 0); }
            }
            if constexpr (g < 32) soft(hh, T, j, 1);
            SBAR();
            if constexpr (g < 16) {
                qk(1, qa >> 1, qa & 1);
                if constexpr (qn == 2) qk(1, (qa + 1) >> 1, (qa + 1) & 1);
                if constexpr (g == 0) ldk(1, 2, 0);
                if constexpr (g == 2) { merge(1, 0); merge(1, 1); }
                if constexpr (g == 3) { tmax(1, 0, 0); tmax(1, 0, 1); }
                if constexpr (g >= 4) tmax(1, (g - 4) / 2 + 1, j);
            }
            if constexpr (g < 32 && j == 0 && T < 7) shift(hh, T + 1);
            SBAR();
        });
        outp(1);
        signal<0>(L_CNT + 4, lane, nonce, rws, WS_FO + (rb * 16 + hp) * FS);
        if (blockIdx.x == 0 && wid == 4) {
#pragma unroll 1
            for (int j = 0; j < 4; ++j) poll(ws, WS_FQ + (j * 64 + lane) * FS, nonce);
            st32((nonce - 0x7F4A7C15u) * 0x0E8B2F51u + 1u, rws, lane == 0 ? 0 : OOB, WS_E);
        }
        asm volatile(".Lfk_end:");
        return;
    }
    const int mw = wid;
    const unsigned abase = WS_ACT + (rt * 68 + mw) * 4096, wbase = WS_WT + (ct * 68 + mw) * 5120;
    unsigned ab[4];
#pragma unroll
    for (int t = 0; t < 4; ++t) ab[t] = abase + ((t + mw) & 3) * 1024;
    unsigned vo = lane * 16;
    OPAQUE(vo);
    bf16x8 W[17][5], A[17][4];
#define LDW(i) _Pragma("unroll") for (int n = 0; n < 5; ++n) W[i][n] = ld128(rws, vo + (n & 3) * 1024, wbase + (i) * 20480 + (n >> 2) * 4096)
#define LDA(i) _Pragma("unroll") for (int t = 0; t < 4; ++t) A[i][t] = ld128(rws, vo, ab[t] + (i) * 16384)
    {
        const bf16x8 rt0 = cat(trread(c.brt), trread(c.brt + 2048)), rt1 = cat(trread(c.brt + 4096), trread(c.brt + 6144));
        unsigned vh[2][3];
#pragma unroll
        for (int i = 0; i < 3; ++i) vh[1][i] = vhof(wid * 64 + lane + 512 * i + 256, c.cg, c.par);
        vh[0][0] = c.vh0; vh[0][1] = c.vh1; vh[0][2] = c.vh2;
        st128(__builtin_bit_cast(u32x4, rt0), sws, vo, c.rd0);
        st128(__builtin_bit_cast(u32x4, rt1), sws, vo, c.rd1);
#pragma unroll
        for (int t = 12; t < 15; ++t)
#pragma unroll
            for (int j = 0; j < 2; ++j) dma16(c.sh, vh[j][t % 3], (c.rot == 0 ? 4 : c.rot - 1) * (128 * 3072), t * 8192 + wid * 1024 + j * 4096);
        if (lane < 8) *(LAS unsigned*)(uintptr_t)(L_CNT + lane * 4) = 0u;
        wait_bar_lgkm<8>();
        st32(nonce, rws, wid == 0 && lane == 0 ? 0 : OOB, WS_FQ + (c.rb * 16 + c.cg) * FS);
        signal<1>(L_CNT + 16, lane, nonce, rws, c.cg < 15 ? WS_FW + (c.rb * 15 + c.cg) * FS : OOB);
    }
    unsigned wf0;
    {
        const int i = lane < 34 ? lane >> 1 : 0, ng = (5 * ct + 4 * (lane & 1)) >> 2, ks = mw + 4 * i;
        const int ir = ((ng * 4 + ks - 64) * 43) >> 7;
        wf0 = ks < 48 ? WS_FW + ((ks * 10 + ng) >> 1) * FS : WS_FQ + ((ks < 64 ? ks - 48 : ir) * 16 + 15) * FS;
    }
    OPAQUE(wf0);
    if (WPRE) {
        if (lane < 2 * WPRE) poll(ws, wf0, nonce);
#pragma unroll
        for (int i = 0; i < WPRE; ++i) { LDW(i); }
        SBAR();
    }
    {
        const int kind = (lane >> 1) & 3, dr = (lane >> 3) & 3;
        int rr = rb - ((kind == 1 || kind == 2) && dr < 3 ? dr : 0); rr = rr < 0 ? 0 : rr;
        const int cc = kind == 3 ? 15 : 5 * kind + ((sb + 2 * (lane & 1)) >> 3);
        poll(ws, WS_FQ + (rr * 16 + cc) * FS, nonce);
    }
    {
        u32x4 sg[21];
        unsigned tpo[3];
#pragma unroll
        for (int j = 0; j < 3; ++j) { int gtp = 4 * rb - 8 + 4 * j + mw; gtp = gtp < 0 ? 0 : gtp; tpo[j] = WS_QKV + (sb * 64 + gtp) * 1024; }
#pragma unroll
        for (int i = 0; i < 9; ++i) { sg[i] = __builtin_bit_cast(u32x4, ld128(rws, vo, tpo[i % 3] + (40 + i / 3) * 65536)); if (PACE) __builtin_amdgcn_s_sleep(PACE); SBAR(); }
#pragma unroll
        for (int i = 0; i < 3; ++i) { sg[9 + i] = __builtin_bit_cast(u32x4, ld128(rws, vo, tpo[2] + i * 65536)); if (PACE && i < 2) __builtin_amdgcn_s_sleep(PACE); SBAR(); }
        SBAR();
        const unsigned gt = __builtin_amdgcn_raw_buffer_load_b32(rws, (lane & 31) * 4, WS_G + (rb * 128 + mw * 32) * 4, SC1);
        const unsigned fw0 = __builtin_amdgcn_raw_buffer_load_b32(rws, wf0, 0, SC1);
        SBAR();
#define LDV(i) sg[12 + (i)] = __builtin_bit_cast(u32x4, ld128(rws, vo, tpo[(i) % 3] + (80 + (i) / 3) * 65536))
#pragma unroll
        for (int i = 0; i < VN0; ++i) LDV(i);
        SBAR();
        const unsigned la = mw * 1024 + lane * 16;
#pragma unroll
        for (int i = 0; i < 9; ++i) {
            *(LAS u32x4*)(uintptr_t)(la + L_K + i * 4096) = sg[i];
            if (i >= VPOS && VN0 + i - VPOS < 9) { SBAR(); LDV(VN0 + i - VPOS); SBAR(); }
        }
#pragma unroll
        for (int i = 0; i < 3; ++i) *(LAS u32x4*)(uintptr_t)(la + L_Q + i * 4096) = sg[9 + i];
        *(LAS unsigned*)(uintptr_t)(L_GT + mw * 128 + (lane & 31) * 4) = gt;
        lds_count(L_CNT + 8, lane);
        SBAR();
#pragma unroll
        for (int i = VN0 + (VPOS < 9 ? 9 - VPOS : 0); i < 9; ++i) LDV(i);
        SBAR();
        if (!__all(fw0 == nonce)) poll(ws, wf0, nonce);
#pragma unroll
        for (int i = WPRE; i < WPRE + WEARLY && i < 5; ++i) { LDW(i); }
        SBAR();
        if (SB12 < 52) lds_wait(L_CNT + 20);
        const bool one0 = odd && r == 0, one2 = !odd && r == 15;
#pragma unroll
        for (int i = 0; i < 9; ++i) {
            u32x4 v = sg[12 + i];
            if (i < 3 || i >= 6) {
#pragma unroll
                for (int e = 0; e < 4; ++e) v[e] = (i < 3 ? one0 : one2) ? 0x3f803f80u : v[e];
            }
            *(LAS u32x4*)(uintptr_t)(la + L_V + i * 4096) = v;
        }
    }
    lds_count(L_CNT + 12, lane);
    OPAQUE(vo);
    {
        const int l = (vo >> 4) & 15;
        const unsigned fho = l < 12 ? WS_FH + ((rt >> 1) * 16 + (((mw + 4 * l) * 43) >> 7)) * FS : WS_FQ + ((rt >> 1) * 16 + 15) * FS;
        unsigned f1 = 0, f2 = 0;
        if (GPRIO) __builtin_amdgcn_s_setprio(GPRIO);
        if (HWT) { lds_wait(L_CNT); if (HWT > 1) __builtin_amdgcn_s_sleep(HWT); }
#pragma unroll
        for (int i = WPRE + WEARLY; i < 5 + NPK + 1; ++i) {
            if (i == FP1) { SBAR(); asm volatile("s_waitcnt vmcnt(%0)" :: "n"(FWT) : "memory"); f1 = __builtin_amdgcn_raw_buffer_load_b32(rws, fho, 0, SC1); SBAR(); }
            if (i == FP2) { SBAR(); f2 = __builtin_amdgcn_raw_buffer_load_b32(rws, fho, 0, SC1); SBAR(); }
            if (i < 5 + NPK) { LDW(i); } else { LDW(16); }
        }
        SBAR();
        if (FP1 > 20 || !__all(f1 == nonce)) {
            if (FP2 > 20 || !__all(f2 == nonce)) poll(ws, fho, nonce);
        }
    }
    f32x4 acc[4][5];
    const f32x4 zero4 = {0.f, 0.f, 0.f, 0.f};
#define MMA(i) _Pragma("unroll") for (int n = 0; n < 5; ++n) _Pragma("unroll") for (int t = 0; t < 4; ++t) acc[t][n] = MFMA(W[i][n], A[i][t], (i) == 0 ? zero4 : acc[t][n])
    const unsigned pk = L_RED + mw * 15360 + vo;
    LDA(0); LDA(1);
    SBAR();
    if (!HWT) lds_wait(L_CNT);
#pragma unroll
    for (int j = 0; j < 5 * NPK + 5; ++j) *(LAS bf16x8*)(uintptr_t)(pk + j * 1024) = W[j < 5 * NPK ? 5 + j / 5 : 16][j % 5];
    asm volatile("" ::: "memory");
    SBAR();
    LDA(2); LDA(3);
    float gate[4];
    unsigned fo, foo;
    SBAR();
#pragma unroll
    for (int i = 0; i < 12; ++i) {
#pragma unroll
        for (int n = 0; n < 5; ++n) {
#pragma unroll
            for (int t = 0; t < 4; ++t) acc[t][n] = MFMA(W[i][n], A[i][t], i == 0 ? zero4 : acc[t][n]);
            SBAR();
            const int j = 5 * i + n + LEAD, jq = j / 5, jn = j % 5;
            const int ji = jq < 8 ? jq + NPK : jq < 8 + NPK ? jq - 3 : jq;
            if (j >= 25 && j < 80) {
                if (ji < 5 + NPK) W[ji][jn] = ldsr128(pk + ((ji - 5) * 5 + jn) * 1024);
                else W[ji][jn] = ld128(rws, vo + (jn & 3) * 1024, wbase + ji * 20480 + (jn >> 2) * 4096);
            }
            SBAR();
        }
        if (i + 4 < 12) { LDA(i + 4); }
        if (i == FOI) {
            OPAQUE(vo);
            const int l = (vo >> 4) & 7;
            foo = WS_FO + ((rt >> 1) * 16 + (l < 4 ? mw + 4 * l : 4 * mw + l - 4)) * FS;
            fo = __builtin_amdgcn_raw_buffer_load_b32(rws, foo, 0, SC1);
        }
        if (i == 8) {
            OPAQUE(vo);
#pragma unroll
            for (int t = 0; t < 4; ++t) gate[t] = __uint_as_float(__builtin_amdgcn_raw_buffer_load_b32(rws, (vo >> 2) & 60, WS_G + (rt * 64 + ((t + mw) & 3) * 16) * 4, SC1));
        }
        SBAR();
    }
    if (!__all(fo == nonce)) poll(ws, foo, nonce);
    OPAQUE(vo);
    LDA(12); LDA(13); LDA(14); LDA(15); SBAR();
#pragma unroll
    for (int t = 0; t < 4; ++t) {
        const float og = bfround(1.0f - gate[t]);
#pragma unroll
        for (int n = 0; n < 5; ++n) { acc[t][n] *= og; OPAQUE(acc[t][n]); }
    }
    SBAR();
    LDA(16); SBAR();
    MMA(12); SBAR();
#pragma unroll
    for (int n = 0; n < 5; ++n) W[16][n] = ldsr128(L_RED + mw * 15360 + vo + (5 * NPK + n) * 1024);
    SBAR();
    MMA(13); SBAR(); MMA(14); SBAR(); MMA(15); SBAR();
#pragma unroll
    for (int t = 3; t >= 0; --t)
#pragma unroll
        for (int n = 0; n < 5; ++n) acc[t][n] = MFMA(W[16][n], A[16][t], acc[t][n]);
    OPAQUE(vo);
#pragma unroll
    for (int t = 1; t < 4; ++t)
#pragma unroll
        for (int n = 0; n < 5; ++n) *(LAS f32x4*)(uintptr_t)(L_RED + mw * 15360 + vo + ((t - 1) * 5 + n) * 1024) = acc[t][n];
    u16* op = (u16*)mkptr(ka[14], ka[15]) + (size_t)(rt * 64 + mw * 16 + ((vo >> 4) & 15)) * DM + ct * 80 + (vo >> 8) * 4;
    unsigned ra[3];
#pragma unroll
    for (int t = 1; t < 4; ++t) ra[t - 1] = L_RED + ((mw - t) & 3) * 15360 + vo + (t - 1) * 5120;
    asm volatile("" : "+v"(op), "+v"(ra[0]), "+v"(ra[1]), "+v"(ra[2]), "+v"(acc[0][0]), "+v"(acc[0][1]), "+v"(acc[0][2]), "+v"(acc[0][3]), "+v"(acc[0][4]));
    asm volatile("s_waitcnt lgkmcnt(0)\n\ts_barrier" ::: "memory");
    f32x4 fin[5], pr[5][3];
#pragma unroll
    for (int n = 0; n < 5; ++n)
#pragma unroll
        for (int t = 0; t < 3; ++t) pr[n][t] = *(LAS f32x4*)(uintptr_t)(ra[t] + n * 1024);
    SBAR();
#pragma unroll
    for (int n = 0; n < 5; ++n) fin[n] = (acc[0][n] + pr[n][0]) + (pr[n][1] + pr[n][2]);
#pragma unroll
    for (int n = 0; n < 5; ++n) *(GAS u32x2*)(op + n * 16) = (u32x2){pk2(fin[n][0], fin[n][1]), pk2(fin[n][2], fin[n][3])};
}

extern "C" void kernel_launch(void* const* d_in, const int* in_sizes, int n_in, void* d_out, int out_size, void* d_ws, size_t ws_size, hipStream_t stream) {
    fused_kernel<<<256, 512, 0, stream>>>((const u16*)d_in[0], (const u16*)d_in[1], (const u16*)d_in[4], (const u16*)d_in[5], (const u16*)d_in[2], (const u16*)d_in[6], (char*)d_ws, (u16*)d_out);
}
```
